```python
import torch

def _softmax(x, axis=-1):
    return torch.softmax(x, dim=axis)
def _gelu(x):
    return torch.nn.functional.gelu(x, approximate="tanh")
def _silu(x):
    return torch.nn.functional.silu(x)
def _sigmoid(x):
    return torch.sigmoid(x)
def _layernorm(x, axis=-1, eps=1e-5):
    m = x.mean(dim=axis, keepdim=True)
    v = x.var(dim=axis, keepdim=True, unbiased=False)
    return (x - m) / torch.sqrt(v + eps)
def _scatter_add(tgt, idx, src, axis=0):
    return tgt.clone().index_add_(axis, idx, src)
def _segment_sum(src, seg, n):
    out = torch.zeros((n,) + src.shape[1:], dtype=src.dtype)
    return out.index_add_(0, seg, src)
def _pad(x, pw):
    flat = tuple(v for pair in reversed(pw) for v in pair)
    return torch.nn.functional.pad(x, flat)

kernel_name = 'sequential_preLN__gqa_block_cached__mixer_token_mlp__decode'

def setup_inputs():

    _w = (lambda a: (lambda t: t.to(torch.bfloat16) if t.is_floating_point() else t)(torch.as_tensor(a)))
    def _A_setup():
        B, Sq, D = 1, 1, 512
        Hq, Hkv, Sk, Dh = 32, 8, 65536, 128
        assert Hq % Hkv == 0, (Hq, Hkv)
        return {
            "x": _w((lambda *s: torch.randn(*s, dtype=torch.bfloat16) * 0.1)(B, Sq, D)),
            "k_cache": _w((lambda *s: torch.randn(*s, dtype=torch.bfloat16) * 1.6)(B, Hkv, Sk, Dh)),
            "v_cache": _w((lambda *s: torch.randn(*s, dtype=torch.bfloat16) * 1.6)(B, Hkv, Sk, Dh)),
            "wq": _w((lambda *s: torch.randn(*s, dtype=torch.bfloat16) * 1.6)(D, Hq * Dh) * (D ** -0.5)),
            "wo": _w((lambda *s: torch.randn(*s, dtype=torch.bfloat16) * 1.6)(Hq * Dh, D) * ((Hq * Dh) ** -0.5)),
        }
    _A_in = _A_setup()

    def _B_setup():
        B, S, D, F = 1, 1, 512, 1792
        return {
            "x": _w((lambda *s: torch.randn(*s, dtype=torch.bfloat16) * 0.1)(B, S, D)),
            "w1": _w((lambda *s: torch.randn(*s, dtype=torch.bfloat16) * 1.6)(S, F) * (S ** -0.5)),
            "b1": _w((lambda *s: torch.randn(*s, dtype=torch.bfloat16) * 1.6)(F) * 0.02),
            "w2": _w((lambda *s: torch.randn(*s, dtype=torch.bfloat16) * 1.6)(F, S) * (F ** -0.5)),
            "b2": _w((lambda *s: torch.randn(*s, dtype=torch.bfloat16) * 1.6)(S) * 0.02),
        }
    _B_in = _B_setup()

    return {
        "A_x": _A_in["x"],
        "A_k_cache": _A_in["k_cache"],
        "A_v_cache": _A_in["v_cache"],
        "A_wq": _A_in["wq"],
        "A_wo": _A_in["wo"],
        "B_x": _B_in["x"],
        "B_w1": _B_in["w1"],
        "B_b1": _B_in["b1"],
        "B_w2": _B_in["w2"],
        "B_b2": _B_in["b2"],
    }

def reference(A_x, A_k_cache, A_v_cache, A_wq, A_wo, B_x, B_w1, B_b1, B_w2, B_b2):
    def _A_ref(x, k_cache, v_cache, wq, wo):
        B, Sq, D = x.shape
        Hq, Dh = 32, 128
        Hkv = k_cache.shape[1]
        rep = Hq // Hkv
        q = (lambda x, *d: x.permute(*d))((x @ wq).reshape(B, Sq, Hq, Dh), 0, 2, 1, 3)
        k = (lambda x, n, axis: torch.repeat_interleave(x, n, dim=axis))(k_cache, rep, axis=1)
        v = (lambda x, n, axis: torch.repeat_interleave(x, n, dim=axis))(v_cache, rep, axis=1)
        scores = torch.einsum("bhqd,bhkd->bhqk", q, k) * (Dh ** -0.5)
        p = _softmax(scores, axis=-1)
        o = torch.einsum("bhqk,bhkd->bhqd", p, v)
        return (lambda x, *d: x.permute(*d))(o, 0, 2, 1, 3).reshape(B, Sq, Hq * Dh) @ wo

    def _B_ref(x, w1, b1, w2, b2):
        B, S, D = x.shape
        h = _layernorm(x, axis=-1)
        u = torch.einsum("bsd,sf->bdf", h, w1) + b1
        u = _gelu(u)
        y = torch.einsum("bdf,fs->bds", u, w2) + b2
        return (lambda x, *d: x.permute(*d))(y, 0, 2, 1)

    x = A_x
    h = x + _A_ref(_layernorm(x, axis=-1), A_k_cache, A_v_cache, A_wq, A_wo)
    h2 = h + _B_ref(_layernorm(h, axis=-1), B_w1, B_b1, B_w2, B_b2)
    return h2
```

```cpp
#include <hip/hip_runtime.h>
#include <hip/hip_bf16.h>
#include <cstdint>
#include <type_traits>

#define MIMIC 1
#define WIN 32

typedef unsigned v4u __attribute__((ext_vector_type(4)));
typedef float f32x4 __attribute__((ext_vector_type(4)));
typedef __bf16 bf16x8 __attribute__((ext_vector_type(8)));
typedef unsigned short u16;

constexpr int D = 512, HQ = 32, HKV = 8, SK = 65536, DH = 128, FF = 1792;
constexpr int NSPLIT = 32, NWAVE = 8, NTHR = 512;
constexpr int KEYS_WG = SK / NSPLIT;
constexpr int KEYS_WAVE = KEYS_WG / NWAVE;
constexpr int NLK = KEYS_WAVE / 4;
constexpr int NL = 2 * NLK;
constexpr float EPS = 1e-5f;
constexpr float SCALE = 0.08838834764831845f;
constexpr float LOG2E = 1.4426950408889634f;

constexpr size_t WS_Q = 0;
constexpr size_t WS_PML = 16384;
constexpr size_t WS_PO = 32768;
constexpr size_t WS_Z = WS_PO + 256 * 512 * 4;

__device__ __forceinline__ float bf2f(u16 b) { return __uint_as_float((unsigned)b << 16); }
__device__ __forceinline__ float rbf(float x) {
  return (float)(__bf16)x;
}
__device__ __forceinline__ u16 f2bf(float x) { return __builtin_bit_cast(u16, (__bf16)x); }
__device__ __forceinline__ float lo16(unsigned w) { return __uint_as_float(w << 16); }
__device__ __forceinline__ float hi16(unsigned w) { return __uint_as_float(w & 0xffff0000u); }
__device__ __forceinline__ unsigned cvtpk(float lo, float hi) {
  typedef __bf16 bf2 __attribute__((ext_vector_type(2)));
  bf2 r = {(__bf16)lo, (__bf16)hi};
  return __builtin_bit_cast(unsigned, r);
}
__device__ __forceinline__ f32x4 mfma(v4u a, v4u b, f32x4 c) {
  return __builtin_amdgcn_mfma_f32_16x16x32_bf16(__builtin_bit_cast(bf16x8, a), __builtin_bit_cast(bf16x8, b), c, 0, 0, 0);
}
template <int CTRL> __device__ __forceinline__ float dpp(float x) {
  return __builtin_bit_cast(float, __builtin_amdgcn_mov_dpp(__builtin_bit_cast(int, x), CTRL, 0xf, 0xf, true));
}
constexpr int XOR1 = 0xB1, XOR2 = 0x4E, ROR8 = 0x128, ROR4 = 0x124;
__device__ __forceinline__ float xrow_sum(float x) {
  auto s = __builtin_amdgcn_permlane16_swap(__float_as_uint(x), __float_as_uint(x), false, false);
  x = __uint_as_float(s[0]) + __uint_as_float(s[1]);
  auto t = __builtin_amdgcn_permlane32_swap(__float_as_uint(x), __float_as_uint(x), false, false);
  return __uint_as_float(t[0]) + __uint_as_float(t[1]);
}
__device__ __forceinline__ float xrow_max(float x) {
  auto s = __builtin_amdgcn_permlane16_swap(__float_as_uint(x), __float_as_uint(x), false, false);
  x = fmaxf(__uint_as_float(s[0]), __uint_as_float(s[1]));
  auto t = __builtin_amdgcn_permlane32_swap(__float_as_uint(x), __float_as_uint(x), false, false);
  return fmaxf(__uint_as_float(t[0]), __uint_as_float(t[1]));
}
__device__ __forceinline__ float wave_sum(float x) {
  x += dpp<XOR1>(x); x += dpp<XOR2>(x); x += dpp<ROR4>(x); x += dpp<ROR8>(x);
  return xrow_sum(x);
}
__device__ __forceinline__ void wave_ln(float (&v)[8]) {
  float s = 0.f;
#pragma unroll
  for (int i = 0; i < 8; ++i) s += v[i];
  const float mean = wave_sum(s) * (1.f / D);
  float q = 0.f;
#pragma unroll
  for (int i = 0; i < 8; ++i) { const float d = v[i] - mean; q += d * d; }
  const float var = wave_sum(q) * (1.f / D);
  const float mb = rbf(mean), sd = rbf(sqrtf(rbf(rbf(var) + EPS)));
#pragma unroll
  for (int i = 0; i < 8; ++i) v[i] = rbf(rbf(v[i] - mb) / sd);
}

__global__ void __launch_bounds__(512) k_qproj(const u16* __restrict__ x, const u16* __restrict__ wq, u16* __restrict__ q) {
  __shared__ float xn[D];
  __shared__ float red[8][16];
  const int tid = threadIdx.x, lane = tid & 63, w = tid >> 6;
  const int g = blockIdx.x & 7, s = blockIdx.x >> 3, col0 = 512 * g + 16 * s;
  const int half = tid & 1, r0 = tid >> 1;
  const v4u wa = *(const v4u*)(wq + (size_t)r0 * 4096 + col0 + 8 * half);
  const v4u wb = *(const v4u*)(wq + (size_t)(r0 + 256) * 4096 + col0 + 8 * half);
  if (w == 0) {
    const v4u xv = *(const v4u*)(x + 8 * lane);
    float v[8];
#pragma unroll
    for (int i = 0; i < 4; ++i) { v[2 * i] = lo16(xv[i]); v[2 * i + 1] = hi16(xv[i]); }
    wave_ln(v);
#pragma unroll
    for (int i = 0; i < 8; ++i) xn[8 * lane + i] = v[i];
  }
  __syncthreads();
  const float xa = xn[r0], xb = xn[r0 + 256];
  float a[8];
#pragma unroll
  for (int i = 0; i < 4; ++i) {
    a[2 * i] = xa * lo16(wa[i]) + xb * lo16(wb[i]);
    a[2 * i + 1] = xa * hi16(wa[i]) + xb * hi16(wb[i]);
  }
#pragma unroll
  for (int i = 0; i < 8; ++i) {
    float t = a[i];
    t += dpp<XOR2>(t); t += dpp<ROR4>(t); t += dpp<ROR8>(t);
    a[i] = xrow_sum(t);
  }
  if (lane < 2) {
#pragma unroll
    for (int i = 0; i < 8; ++i) red[w][8 * lane + i] = a[i];
  }
  __syncthreads();
  if (tid < 16) {
    float t = 0.f;
#pragma unroll
    for (int i = 0; i < 8; ++i) t += red[i][tid];
    q[col0 + tid] = f2bf(t);
  }
}

constexpr int AUX_NT = 2;
template <int I> __device__ __forceinline__ v4u ldkv(__amdgpu_buffer_rsrc_t rk, __amdgpu_buffer_rsrc_t rv, int koff, int voff) {
  if constexpr (I < NLK) return __builtin_amdgcn_raw_buffer_load_b128(rk, koff + (I & 3) * 1024, (I >> 2) * 4096, AUX_NT);
  else return __builtin_amdgcn_raw_buffer_load_b128(rv, voff + ((I - NLK) & 3) * 1024, ((I - NLK) >> 2) * 4096, AUX_NT);
}
template <int BM> __device__ __forceinline__ float bsel(float old, float src) {
  return __builtin_bit_cast(float, __builtin_amdgcn_update_dpp(__builtin_bit_cast(int, old), __builtin_bit_cast(int, src), 0xE4, 0xf, BM, false));
}
template <int I, int N> struct Loop {
  template <class F> static __device__ __forceinline__ void run(F&& f) {
    if constexpr (I < N) { f(std::integral_constant<int, I>{}); Loop<I + 1, N>::run(f); }
  }
};
__global__ void __launch_bounds__(NTHR, 2) k_attn(const u16* __restrict__ q, const u16* __restrict__ kc, const u16* __restrict__ vc,
                                                  float* __restrict__ pml, float* __restrict__ po) {
  __shared__ __attribute__((aligned(16))) float lds[NWAVE * 512 + 64];
  const int tid = threadIdx.x, lane = tid & 63, w = __builtin_amdgcn_readfirstlane(tid >> 6);
  const int g = blockIdx.x & 7, s = blockIdx.x >> 3;
  const size_t key0 = (size_t)g * SK + (size_t)s * KEYS_WG + (size_t)w * KEYS_WAVE;
  const int lg = lane >> 4, bank = (lane >> 2) & 3, lh = lane & 3;
  const __amdgpu_buffer_rsrc_t rk = __builtin_amdgcn_make_buffer_rsrc((void*)(kc + key0 * DH), 0, KEYS_WAVE * DH * 2, 0x00020000);
  const __amdgpu_buffer_rsrc_t rv = __builtin_amdgcn_make_buffer_rsrc((void*)(vc + key0 * DH), 0, KEYS_WAVE * DH * 2, 0x00020000);
  const int koff = 16 * (16 * bank + 4 * lg + lh), voff = 16 * lane;
  const v4u qb = *(const v4u*)(q + (4 * g + lh) * DH + 8 * (4 * lg + bank));
  v4u ring[WIN];
  Loop<0, WIN>::run([&](auto I) { ring[I.value] = ldkv<I.value>(rk, rv, koff, voff); });
  __builtin_amdgcn_sched_barrier(0);
  float X[NLK / 4];
  Loop<0, NLK / 4>::run([&](auto T) {
    constexpr int t = T.value;
    f32x4 d[4];
    Loop<0, 4>::run([&](auto M) {
      constexpr int i = 4 * t + M.value;
      d[M.value] = mfma(ring[i % WIN], qb, f32x4{0.f, 0.f, 0.f, 0.f});
      ring[i % WIN] = ldkv<i + WIN>(rk, rv, koff, voff);
    });
    float R[4];
#pragma unroll
    for (int k = 0; k < 4; ++k) R[k] = bsel<8>(bsel<4>(bsel<2>(d[k][0], d[(k + 1) & 3][1]), d[(k + 2) & 3][2]), d[(k + 3) & 3][3]);
    X[t] = (R[0] + dpp<ROR4>(R[1])) + (dpp<ROR8>(R[2]) + dpp<0x12C>(R[3]));
    __builtin_amdgcn_sched_barrier(0);
  });
  float m = -INFINITY;
#pragma unroll
  for (int t = 0; t < NLK / 4; ++t) { X[t] = rbf(rbf(X[t]) * SCALE); m = fmaxf(m, X[t]); }
  m = fmaxf(m, dpp<ROR4>(m)); m = fmaxf(m, dpp<ROR8>(m)); m = xrow_max(m);
  float* mx = lds + NWAVE * 512;
  if (lane < 4) mx[w * 4 + lane] = m;
  __syncthreads();
#pragma unroll
  for (int i = 0; i < NWAVE; ++i) m = fmaxf(m, mx[i * 4 + lh]);
  float l = 0.f;
#pragma unroll
  for (int t = 0; t < NLK / 4; ++t) { X[t] = __builtin_amdgcn_exp2f((X[t] - m) * LOG2E); l += X[t]; }
  unsigned PK[NLK / 8];
#pragma unroll
  for (int t = 0; t < NLK / 8; ++t) PK[t] = cvtpk(X[2 * t], X[2 * t + 1]);
  unsigned bm[4];
#pragma unroll
  for (int mm = 0; mm < 4; ++mm) bm[mm] = bank == mm ? 0xffffffffu : 0u;
  f32x4 acc[8];
#pragma unroll
  for (int j = 0; j < 8; ++j) acc[j] = f32x4{0.f, 0.f, 0.f, 0.f};
  __builtin_amdgcn_sched_barrier(0);
  Loop<0, NLK / 8>::run([&](auto TT) {
    constexpr int T = TT.value;
    constexpr int base = NLK + 8 * T;
    v4u pa;
#pragma unroll
    for (int mm = 0; mm < 4; ++mm) pa[mm] = PK[T] & bm[mm];
    v4u b[8];
#pragma unroll
    for (int jj = 0; jj < 4; ++jj)
#pragma unroll
      for (int mm = 0; mm < 4; ++mm) {
        b[2 * jj][mm] = __builtin_amdgcn_perm(ring[(base + 4 + mm) % WIN][jj], ring[(base + mm) % WIN][jj], 0x05040100u);
        b[2 * jj + 1][mm] = __builtin_amdgcn_perm(ring[(base + 4 + mm) % WIN][jj], ring[(base + mm) % WIN][jj], 0x07060302u);
      }
    Loop<0, 8>::run([&](auto I) {
      constexpr int idx = base + I.value;
      if constexpr (idx + WIN < NL) ring[idx % WIN] = ldkv<idx + WIN>(rk, rv, koff, voff);
    });
#pragma unroll
    for (int j = 0; j < 8; ++j) acc[j] = mfma(pa, b[j], acc[j]);
    __builtin_amdgcn_sched_barrier(0);
  });
  l += dpp<ROR4>(l); l += dpp<ROR8>(l); l = xrow_sum(l);
  float r1[16];
#pragma unroll
  for (int j = 0; j < 8; ++j)
#pragma unroll
    for (int hh = 0; hh < 2; ++hh) {
      auto sw = __builtin_amdgcn_permlane32_swap(__float_as_uint(acc[j][hh]), __float_as_uint(acc[j][hh + 2]), false, false);
      r1[2 * j + hh] = __uint_as_float(sw[0]) + __uint_as_float(sw[1]);
    }
  float r2[8];
#pragma unroll
  for (int j = 0; j < 8; ++j) {
    auto sw = __builtin_amdgcn_permlane16_swap(__float_as_uint(r1[2 * j]), __float_as_uint(r1[2 * j + 1]), false, false);
    r2[j] = __uint_as_float(sw[0]) + __uint_as_float(sw[1]);
  }
  *(f32x4*)(lds + w * 512 + lane * 8) = f32x4{r2[0], r2[1], r2[2], r2[3]};
  *(f32x4*)(lds + w * 512 + lane * 8 + 4) = f32x4{r2[4], r2[5], r2[6], r2[7]};
  if (lane < 4) mx[32 + w * 4 + lane] = l;
  __syncthreads();
  float o = 0.f;
#pragma unroll
  for (int i = 0; i < NWAVE; ++i) o += lds[i * 512 + tid];
  const int wg = g * NSPLIT + s;
  po[wg * 512 + tid] = o;
  if (tid < 4) {
    float L = 0.f;
#pragma unroll
    for (int i = 0; i < NWAVE; ++i) L += mx[32 + i * 4 + tid];
    pml[(wg * 4 + tid) * 2] = m;
    pml[(wg * 4 + tid) * 2 + 1] = L;
  }
}

__global__ void __launch_bounds__(512) k_comb(const float* __restrict__ pml, const float* __restrict__ po, const u16* __restrict__ wo,
                                              float* __restrict__ z) {
  __shared__ float osh[4][128];
  __shared__ float ob[128];
  __shared__ float zr[8][512];
  const int tid = threadIdx.x, lane = tid & 63, w = tid >> 6;
  const int h = blockIdx.x, g = h >> 2, hh = h & 3;
  v4u wv[16];
#pragma unroll
  for (int i = 0; i < 16; ++i) wv[i] = *(const v4u*)(wo + (size_t)(h * 128 + 16 * w + i) * D + 8 * lane);
  float M = -INFINITY;
  for (int s = 0; s < NSPLIT; ++s) M = fmaxf(M, pml[((g * NSPLIT + s) * 4 + hh) * 2]);
  float L = 0.f;
  for (int s = 0; s < NSPLIT; ++s) L += pml[((g * NSPLIT + s) * 4 + hh) * 2 + 1] * __builtin_amdgcn_exp2f((pml[((g * NSPLIT + s) * 4 + hh) * 2] - M) * LOG2E);
  const int d = tid & 127, part = tid >> 7;
  float o = 0.f;
#pragma unroll
  for (int s = part * 8; s < part * 8 + 8; ++s)
    o += po[(g * NSPLIT + s) * 512 + hh * 128 + d] * __builtin_amdgcn_exp2f((pml[((g * NSPLIT + s) * 4 + hh) * 2] - M) * LOG2E);
  osh[part][d] = o;
  __syncthreads();
  if (tid < 128) ob[tid] = rbf((osh[0][tid] + osh[1][tid] + osh[2][tid] + osh[3][tid]) / L);
  __syncthreads();
  float a[8] = {0.f, 0.f, 0.f, 0.f, 0.f, 0.f, 0.f, 0.f};
#pragma unroll
  for (int i = 0; i < 16; ++i) {
    const float ov = ob[16 * w + i];
#pragma unroll
    for (int k = 0; k < 4; ++k) { a[2 * k] += ov * lo16(wv[i][k]); a[2 * k + 1] += ov * hi16(wv[i][k]); }
  }
#pragma unroll
  for (int k = 0; k < 8; ++k) zr[w][8 * lane + k] = a[k];
  __syncthreads();
  float t = 0.f;
#pragma unroll
  for (int i = 0; i < 8; ++i) t += zr[i][tid];
  z[h * 512 + tid] = t;
}

__device__ __forceinline__ float gelu_tanh(float u) {
  const float inner = 0.7978845608028654f * (u + 0.044715f * u * u * u);
  const float e = __builtin_amdgcn_exp2f(inner * (2.f * LOG2E));
  const float th = 1.f - 2.f * __builtin_amdgcn_rcpf(e + 1.f);
  return 0.5f * u * (1.f + th);
}

__global__ void __launch_bounds__(512) k_final(const u16* __restrict__ x, const float* __restrict__ z, const u16* __restrict__ w1,
                                               const u16* __restrict__ b1, const u16* __restrict__ w2, const u16* __restrict__ b2,
                                               u16* __restrict__ out) {
  __shared__ float hs[D];
  __shared__ float hn[D];
  __shared__ float red[8];
  const int tid = threadIdx.x, lane = tid & 63, w = tid >> 6;
  float t = 0.f;
#pragma unroll
  for (int i = 0; i < 32; ++i) t += z[i * 512 + tid];
  hs[tid] = rbf(bf2f(x[tid]) + rbf(t));
  __syncthreads();
  if (w == 0) {
    float v[8];
#pragma unroll
    for (int i = 0; i < 8; ++i) v[i] = hs[8 * lane + i];
    wave_ln(v);
    wave_ln(v);
#pragma unroll
    for (int i = 0; i < 8; ++i) hn[8 * lane + i] = v[i];
  }
  __syncthreads();
  const int ch = blockIdx.x * 2 + (tid >> 8), ft = tid & 255;
  const float hv = hn[ch];
  float y = 0.f;
#pragma unroll
  for (int i = 0; i < FF / 256; ++i) {
    const int f = ft + 256 * i;
    const float u = rbf(rbf(hv * bf2f(w1[f])) + bf2f(b1[f]));
    y += rbf(gelu_tanh(u)) * bf2f(w2[f]);
  }
  y = wave_sum(y);
  if (lane == 0) red[w] = y;
  __syncthreads();
  if (tid < 2) {
    const float yy = red[4 * tid] + red[4 * tid + 1] + red[4 * tid + 2] + red[4 * tid + 3];
    const int c = blockIdx.x * 2 + tid;
    out[c] = f2bf(hs[c] + rbf(rbf(yy) + bf2f(b2[0])));
  }
}

extern "C" void kernel_launch(void* const* d_in, const int* in_sizes, int n_in, void* d_out, int out_size, void* d_ws, size_t ws_size,
                              hipStream_t stream) {
  const u16* x = (const u16*)d_in[0];
  const u16* kc = (const u16*)d_in[1];
  const u16* vc = (const u16*)d_in[2];
  const u16* wq = (const u16*)d_in[3];
  const u16* wo = (const u16*)d_in[4];
  const u16* w1 = (const u16*)d_in[6];
  const u16* b1 = (const u16*)d_in[7];
  const u16* w2 = (const u16*)d_in[8];
  const u16* b2 = (const u16*)d_in[9];
  char* ws = (char*)d_ws;
  u16* q = (u16*)(ws + WS_Q);
  float* pml = (float*)(ws + WS_PML);
  float* po = (float*)(ws + WS_PO);
  float* z = (float*)(ws + WS_Z);
  k_qproj<<<256, 512, 0, stream>>>(x, wq, q);
  k_attn<<<256, NTHR, 0, stream>>>(q, kc, vc, pml, po);
  k_comb<<<32, 512, 0, stream>>>(pml, po, wo, z);
  k_final<<<256, 512, 0, stream>>>(x, z, w1, b1, w2, b2, (u16*)d_out);
}
```

```cpp
#include <hip/hip_runtime.h>
#include <hip/hip_bf16.h>
#include <cstdint>
#include <type_traits>

#define WIN 32
#define THR 5
#define DIAG 0

typedef unsigned v4u __attribute__((ext_vector_type(4)));
typedef unsigned v2u __attribute__((ext_vector_type(2)));
typedef float f32x4 __attribute__((ext_vector_type(4)));
typedef __bf16 bf16x8 __attribute__((ext_vector_type(8)));
typedef unsigned short u16;
typedef unsigned long long u64;
#define GAS __attribute__((address_space(1)))
typedef GAS u64 gu64;
#define RLX_AGENT __ATOMIC_RELAXED, __HIP_MEMORY_SCOPE_AGENT

constexpr int D = 512, HKV = 8, SK = 65536, DH = 128, FF = 1792;
constexpr int NSPLIT = 32, NWAVE = 8, NTHR = 512, NWG = 256;
constexpr int KEYS_WG = SK / NSPLIT;
constexpr int KEYS_WAVE = KEYS_WG / NWAVE;
constexpr int NLK = KEYS_WAVE / 4;
constexpr int NL = 2 * NLK;
constexpr float EPS = 1e-5f;
constexpr float SCALE = 0.08838834764831845f;
constexpr float LOG2E = 1.4426950408889634f;
constexpr long long TCAP = 5000000;

constexpr int G_EPOCH = 0;
constexpr int G_Q = 512;
constexpr int G_ML = G_Q + 8 * 512;
constexpr int G_Z = G_ML + 256 * 16;
constexpr int G_ACC = G_Z + 32 * 512;
constexpr int G_END = G_ACC + 256 * 512;
constexpr int G_DIAG = G_END;

__device__ __forceinline__ float bf2f(u16 b) { return __uint_as_float((unsigned)b << 16); }
__device__ __forceinline__ float rbf(float x) { return (float)(__bf16)x; }
__device__ __forceinline__ u16 f2bf(float x) { return __builtin_bit_cast(u16, (__bf16)x); }
__device__ __forceinline__ float lo16(unsigned w) { return __uint_as_float(w << 16); }
__device__ __forceinline__ float hi16(unsigned w) { return __uint_as_float(w & 0xffff0000u); }
__device__ __forceinline__ unsigned cvtpk(float lo, float hi) {
  typedef __bf16 bf2 __attribute__((ext_vector_type(2)));
  bf2 r = {(__bf16)lo, (__bf16)hi};
  return __builtin_bit_cast(unsigned, r);
}
__device__ __forceinline__ f32x4 mfma(v4u a, v4u b, f32x4 c) {
  return __builtin_amdgcn_mfma_f32_16x16x32_bf16(__builtin_bit_cast(bf16x8, a), __builtin_bit_cast(bf16x8, b), c, 0, 0, 0);
}
template <int CTRL> __device__ __forceinline__ float dpp(float x) {
  return __builtin_bit_cast(float, __builtin_amdgcn_mov_dpp(__builtin_bit_cast(int, x), CTRL, 0xf, 0xf, true));
}
constexpr int XOR1 = 0xB1, XOR2 = 0x4E, ROR8 = 0x128, ROR4 = 0x124, ROR12 = 0x12C;
__device__ __forceinline__ float xrow_sum(float x) {
  auto s = __builtin_amdgcn_permlane16_swap(__float_as_uint(x), __float_as_uint(x), false, false);
  x = __uint_as_float(s[0]) + __uint_as_float(s[1]);
  auto t = __builtin_amdgcn_permlane32_swap(__float_as_uint(x), __float_as_uint(x), false, false);
  return __uint_as_float(t[0]) + __uint_as_float(t[1]);
}
__device__ __forceinline__ float xrow_max(float x) {
  auto s = __builtin_amdgcn_permlane16_swap(__float_as_uint(x), __float_as_uint(x), false, false);
  x = fmaxf(__uint_as_float(s[0]), __uint_as_float(s[1]));
  auto t = __builtin_amdgcn_permlane32_swap(__float_as_uint(x), __float_as_uint(x), false, false);
  return fmaxf(__uint_as_float(t[0]), __uint_as_float(t[1]));
}
__device__ __forceinline__ float wave_sum(float x) {
  x += dpp<XOR1>(x); x += dpp<XOR2>(x); x += dpp<ROR4>(x); x += dpp<ROR8>(x);
  return xrow_sum(x);
}
__device__ __forceinline__ void ln_stats(const float (&v)[8], float& mb, float& sd) {
  float s = 0.f;
#pragma unroll
  for (int i = 0; i < 8; ++i) s += v[i];
  const float mean = wave_sum(s) * (1.f / D);
  float q = 0.f;
#pragma unroll
  for (int i = 0; i < 8; ++i) { const float d = v[i] - mean; q += d * d; }
  const float var = wave_sum(q) * (1.f / D);
  mb = rbf(mean);
  sd = rbf(sqrtf(rbf(rbf(var) + EPS)));
}
__device__ __forceinline__ float ln_apply(float v, float mb, float sd) { return rbf(rbf(v - mb) / sd); }

__device__ __forceinline__ float gelu_tanh(float u) {
  const float inner = 0.7978845608028654f * (u + 0.044715f * u * u * u);
  const float e = __builtin_amdgcn_exp2f(inner * (2.f * LOG2E));
  const float th = 1.f - 2.f * __builtin_amdgcn_rcpf(e + 1.f);
  return 0.5f * u * (1.f + th);
}

struct Tag { unsigned lo, hi8; };
__device__ __forceinline__ void put(gu64* p, float v, Tag t) {
  const u64 x = ((u64)((__float_as_uint(v) & 0xffffff00u) | t.hi8) << 32) | t.lo;
  __hip_atomic_store(p, x, RLX_AGENT);
}
__device__ __forceinline__ u64 get(gu64* p) { return __hip_atomic_load(p, RLX_AGENT); }
__device__ __forceinline__ bool okg(u64 x, Tag t) { return (unsigned)x == t.lo && ((unsigned)(x >> 32) & 0xffu) == t.hi8; }
__device__ __forceinline__ float valg(u64 x) { return __uint_as_float((unsigned)(x >> 32) & 0xffffff00u); }
__device__ __forceinline__ u64 rt() { return __builtin_amdgcn_s_memrealtime(); }

constexpr int AUX_NT = 2;
template <int I> __device__ __forceinline__ v4u ldkv(__amdgpu_buffer_rsrc_t rk, __amdgpu_buffer_rsrc_t rv, int koff, int voff) {
  if constexpr (I < NLK) return __builtin_amdgcn_raw_buffer_load_b128(rk, koff + (I & 3) * 1024, (I >> 2) * 4096, AUX_NT);
  else return __builtin_amdgcn_raw_buffer_load_b128(rv, voff + ((I - NLK) & 3) * 1024, ((I - NLK) >> 2) * 4096, AUX_NT);
}
template <int BM> __device__ __forceinline__ float bsel(float old, float src) {
  return __builtin_bit_cast(float, __builtin_amdgcn_update_dpp(__builtin_bit_cast(int, old), __builtin_bit_cast(int, src), 0xE4, 0xf, BM, false));
}
template <int I, int N> struct Loop {
  template <class F> static __device__ __forceinline__ void run(F&& f) {
    if constexpr (I < N) { f(std::integral_constant<int, I>{}); Loop<I + 1, N>::run(f); }
  }
};
#define STR2(x) #x
#define STR(x) STR2(x)
#define STAMP(k) do { } while (0)

constexpr int L_PART = 0;
constexpr int L_RED = L_PART + 8 * 512;
constexpr int L_MX = L_RED + 8 * 64;
constexpr int L_LS = L_MX + 32;
constexpr int L_SC = L_LS + 32;
constexpr int L_Q = L_SC + 16;
constexpr int L_H = L_Q + 256;
constexpr int L_END = L_H + 512;

__global__ void __launch_bounds__(NTHR, 2) k_fused(const u16* __restrict__ x, const u16* __restrict__ kc, const u16* __restrict__ vc,
                                                   const u16* __restrict__ wq, const u16* __restrict__ wo, const u16* __restrict__ w1,
                                                   const u16* __restrict__ b1, const u16* __restrict__ w2, const u16* __restrict__ b2,
                                                   u16* __restrict__ out, u64* __restrict__ wsp) {
  __shared__ __attribute__((aligned(16))) float lds[L_END];
  gu64* ws = (gu64*)wsp;
  const int tid = threadIdx.x, lane = tid & 63, w = __builtin_amdgcn_readfirstlane(tid >> 6);
  const int g = blockIdx.x & 7, s = blockIdx.x >> 3, wg = g * NSPLIT + s;
  STAMP(0);
  const u64 tstart = rt();
  int zoff = 0;
  asm volatile("" : "+v"(zoff));
  const u64 epoch = get(ws + G_EPOCH + zoff);
  const v4u xv = *(const v4u*)(x + 8 * lane);
  const int r0 = 64 * w + (lane >> 1), col0 = 512 * g + 16 * s + 8 * (lane & 1);
  const u16 xa16 = x[r0], xb16 = x[r0 + 32];
  const v4u wa = *(const v4u*)(wq + (size_t)r0 * 4096 + col0);
  const v4u wb = *(const v4u*)(wq + (size_t)(r0 + 32) * 4096 + col0);
  __builtin_amdgcn_sched_barrier(0);

  const size_t key0 = (size_t)g * SK + (size_t)s * KEYS_WG + (size_t)w * KEYS_WAVE;
  const int lg = lane >> 4, bank = (lane >> 2) & 3, lh = lane & 3;
  const __amdgpu_buffer_rsrc_t rk = __builtin_amdgcn_make_buffer_rsrc((void*)(kc + key0 * DH), 0, KEYS_WAVE * DH * 2, 0x00020000);
  const __amdgpu_buffer_rsrc_t rv = __builtin_amdgcn_make_buffer_rsrc((void*)(vc + key0 * DH), 0, KEYS_WAVE * DH * 2, 0x00020000);
  const int koff = 16 * (16 * bank + 4 * lg + lh), voff = 16 * lane;
  v4u ring[WIN];
  Loop<0, THR + 1>::run([&](auto I) { ring[I.value] = ldkv<I.value>(rk, rv, koff, voff); });
  __builtin_amdgcn_sched_barrier(0);

  u64 mixv = (((u64)__builtin_amdgcn_readfirstlane((unsigned)(epoch >> 32)) << 32) | (unsigned)__builtin_amdgcn_readfirstlane((unsigned)epoch)) + 0x9E3779B97F4A7C15ull;
  mixv = (mixv ^ (mixv >> 30)) * 0xBF58476D1CE4E5B9ull;
  mixv = (mixv ^ (mixv >> 27)) * 0x94D049BB133111EBull;
  mixv ^= mixv >> 31;
  const Tag tag{(unsigned)mixv, (unsigned)(mixv >> 32) & 0xffu};
  {
    float v[8];
#pragma unroll
    for (int i = 0; i < 4; ++i) { v[2 * i] = lo16(xv[i]); v[2 * i + 1] = hi16(xv[i]); }
    float mb, sd;
    ln_stats(v, mb, sd);
    const float xa = ln_apply(bf2f(xa16), mb, sd), xb = ln_apply(bf2f(xb16), mb, sd);
    float a[8];
#pragma unroll
    for (int i = 0; i < 4; ++i) {
      a[2 * i] = xa * lo16(wa[i]) + xb * lo16(wb[i]);
      a[2 * i + 1] = xa * hi16(wa[i]) + xb * hi16(wb[i]);
    }
#pragma unroll
    for (int i = 0; i < 8; ++i) {
      float t = a[i];
      t += dpp<XOR2>(t); t += dpp<ROR4>(t); t += dpp<ROR8>(t);
      a[i] = xrow_sum(t);
    }
    if (lane < 2) {
      *(f32x4*)(lds + L_RED + w * 64 + 8 * lane) = f32x4{a[0], a[1], a[2], a[3]};
      *(f32x4*)(lds + L_RED + w * 64 + 8 * lane + 4) = f32x4{a[4], a[5], a[6], a[7]};
    }
  }
  __syncthreads();
  STAMP(1);
  if (w == 0) {
    if (lane < 16) {
      float t = 0.f;
#pragma unroll
      for (int i = 0; i < NWAVE; ++i) t += lds[L_RED + i * 64 + lane];
      put(ws + G_Q + 512 * g + 16 * s + lane, rbf(t), tag);
    }
    u64 qv[8];
    for (;;) {
      bool ok = true;
#pragma unroll
      for (int i = 0; i < 8; ++i) qv[i] = get(ws + G_Q + 512 * g + 64 * i + lane);
#pragma unroll
      for (int i = 0; i < 8; ++i) ok &= okg(qv[i], tag);
      if (__all(ok)) break;
      if ((long long)(rt() - tstart) > TCAP) break;
    }
#pragma unroll
    for (int i = 0; i < 8; ++i) ((u16*)(lds + L_Q))[64 * i + lane] = (u16)(qv[i] >> 48);
    STAMP(2);
  }
  Loop<THR + 1, WIN>::run([&](auto I) {
    if (w != 0) asm volatile("s_waitcnt vmcnt(" STR(THR) ")" ::: "memory");
    ring[I.value] = ldkv<I.value>(rk, rv, koff, voff);
  });
  __syncthreads();
  STAMP(3);
  const v4u qb = *(const v4u*)((const u16*)(lds + L_Q) + lh * DH + 8 * (4 * lg + bank));
  __builtin_amdgcn_sched_barrier(0);

  float X[NLK / 4];
  Loop<0, NLK / 4>::run([&](auto T) {
    constexpr int t = T.value;
    f32x4 d[4];
    Loop<0, 4>::run([&](auto M) {
      constexpr int i = 4 * t + M.value;
      d[M.value] = mfma(ring[i % WIN], qb, f32x4{0.f, 0.f, 0.f, 0.f});
      ring[i % WIN] = ldkv<i + WIN>(rk, rv, koff, voff);
    });
    float R[4];
#pragma unroll
    for (int k = 0; k < 4; ++k) R[k] = bsel<8>(bsel<4>(bsel<2>(d[k][0], d[(k + 1) & 3][1]), d[(k + 2) & 3][2]), d[(k + 3) & 3][3]);
    X[t] = (R[0] + dpp<ROR4>(R[1])) + (dpp<ROR8>(R[2]) + dpp<ROR12>(R[3]));
    __builtin_amdgcn_sched_barrier(0);
  });
  float m = -INFINITY;
#pragma unroll
  for (int t = 0; t < NLK / 4; ++t) { X[t] = rbf(rbf(X[t]) * SCALE); m = fmaxf(m, X[t]); }
  m = fmaxf(m, dpp<ROR4>(m)); m = fmaxf(m, dpp<ROR8>(m)); m = xrow_max(m);
  if (lane < 4) lds[L_MX + w * 4 + lane] = m;
  __syncthreads();
  STAMP(4);
#pragma unroll
  for (int i = 0; i < NWAVE; ++i) m = fmaxf(m, lds[L_MX + i * 4 + lh]);
  float l = 0.f;
#pragma unroll
  for (int t = 0; t < NLK / 4; ++t) { X[t] = __builtin_amdgcn_exp2f((X[t] - m) * LOG2E); l += X[t]; }
  l += dpp<ROR4>(l); l += dpp<ROR8>(l); l = xrow_sum(l);
  if (lane < 4) lds[L_LS + w * 4 + lane] = l;
  unsigned PK[NLK / 8];
#pragma unroll
  for (int t = 0; t < NLK / 8; ++t) PK[t] = cvtpk(X[2 * t], X[2 * t + 1]);
  __syncthreads();
  if (tid < 4) {
    float L = 0.f;
#pragma unroll
    for (int i = 0; i < NWAVE; ++i) L += lds[L_LS + i * 4 + tid];
    put(ws + G_ML + wg * 16 + 2 * tid, m, tag);
    put(ws + G_ML + wg * 16 + 2 * tid + 1, L, tag);
  }
  const int hh = s & 3, cb = 64 * (s >> 2);
  const int prow = 2 * (tid >> 3);
  const v4u woa = *(const v4u*)(wo + (size_t)((4 * g + hh) * DH + prow) * D + cb + 8 * (tid & 7));
  const v4u wob = *(const v4u*)(wo + (size_t)((4 * g + hh) * DH + prow + 1) * D + cb + 8 * (tid & 7));
  const int fi = tid < FF / 4 ? tid : 0;
  const v2u w1v = *(const v2u*)(w1 + 4 * fi), b1v = *(const v2u*)(b1 + 4 * fi), w2v = *(const v2u*)(w2 + 4 * fi);
  const u16 xt16 = x[tid], b216 = b2[0];

  unsigned bm[4];
#pragma unroll
  for (int mm = 0; mm < 4; ++mm) bm[mm] = bank == mm ? 0xffffffffu : 0u;
  f32x4 acc[8];
#pragma unroll
  for (int j = 0; j < 8; ++j) acc[j] = f32x4{0.f, 0.f, 0.f, 0.f};
  u64 mv[4];
  __builtin_amdgcn_sched_barrier(0);
  Loop<0, NLK / 8>::run([&](auto TT) {
    constexpr int T = TT.value;
    constexpr int base = NLK + 8 * T;
    if constexpr (T == 2) {
#pragma unroll
      for (int i = 0; i < 4; ++i) mv[i] = get(ws + G_ML + (g * NSPLIT + 8 * i + (lane >> 3)) * 16 + (lane & 7));
      __builtin_amdgcn_sched_barrier(0);
    }
    v4u pa;
#pragma unroll
    for (int mm = 0; mm < 4; ++mm) pa[mm] = PK[T] & bm[mm];
    v4u b[8];
#pragma unroll
    for (int jj = 0; jj < 4; ++jj)
#pragma unroll
      for (int mm = 0; mm < 4; ++mm) {
        b[2 * jj][mm] = __builtin_amdgcn_perm(ring[(base + 4 + mm) % WIN][jj], ring[(base + mm) % WIN][jj], 0x05040100u);
        b[2 * jj + 1][mm] = __builtin_amdgcn_perm(ring[(base + 4 + mm) % WIN][jj], ring[(base + mm) % WIN][jj], 0x07060302u);
      }
    Loop<0, 8>::run([&](auto I) {
      constexpr int idx = base + I.value;
      if constexpr (idx + WIN < NL) ring[idx % WIN] = ldkv<idx + WIN>(rk, rv, koff, voff);
    });
#pragma unroll
    for (int j = 0; j < 8; ++j) acc[j] = mfma(pa, b[j], acc[j]);
    __builtin_amdgcn_sched_barrier(0);
    if constexpr (T == 6) {
      if (w == 0) {
        for (;;) {
          bool ok = true;
#pragma unroll
          for (int i = 0; i < 4; ++i) ok &= okg(mv[i], tag);
          if (__all(ok)) break;
          if ((long long)(rt() - tstart) > TCAP) break;
          Loop<0, 4>::run([&](auto I) { mv[I.value] = get(ws + G_ML + (g * NSPLIT + 8 * I.value + (lane >> 3)) * 16 + (lane & 7)); });
        }
        float mx[4], M = -INFINITY;
#pragma unroll
        for (int i = 0; i < 4; ++i) { mx[i] = dpp<0xA0>(valg(mv[i])); M = fmaxf(M, mx[i]); }
        M = fmaxf(M, dpp<ROR8>(M)); M = xrow_max(M);
        float L = 0.f;
#pragma unroll
        for (int i = 0; i < 4; ++i) L += valg(mv[i]) * __builtin_amdgcn_exp2f((mx[i] - M) * LOG2E);
        L += dpp<ROR8>(L); L = xrow_sum(L);
        if (lane < 8 && (lane & 1)) {
          float mw = -INFINITY;
#pragma unroll
          for (int i = 0; i < NWAVE; ++i) mw = fmaxf(mw, lds[L_MX + i * 4 + (lane >> 1)]);
          lds[L_SC + (lane >> 1)] = __builtin_amdgcn_exp2f((mw - M) * LOG2E) / L;
        }
      }
      __builtin_amdgcn_sched_barrier(0);
    }
  });
  STAMP(5);
  {
    float r1[16];
#pragma unroll
    for (int j = 0; j < 8; ++j)
#pragma unroll
      for (int h2 = 0; h2 < 2; ++h2) {
        auto sw = __builtin_amdgcn_permlane32_swap(__float_as_uint(acc[j][h2]), __float_as_uint(acc[j][h2 + 2]), false, false);
        r1[2 * j + h2] = __uint_as_float(sw[0]) + __uint_as_float(sw[1]);
      }
    float r2[8];
#pragma unroll
    for (int j = 0; j < 8; ++j) {
      auto sw = __builtin_amdgcn_permlane16_swap(__float_as_uint(r1[2 * j]), __float_as_uint(r1[2 * j + 1]), false, false);
      r2[j] = __uint_as_float(sw[0]) + __uint_as_float(sw[1]);
    }
    *(f32x4*)(lds + L_PART + w * 512 + lane * 8) = f32x4{r2[0], r2[1], r2[2], r2[3]};
    *(f32x4*)(lds + L_PART + w * 512 + lane * 8 + 4) = f32x4{r2[4], r2[5], r2[6], r2[7]};
  }
  __syncthreads();
  {
    float o = 0.f;
#pragma unroll
    for (int i = 0; i < NWAVE; ++i) o += lds[L_PART + i * 512 + tid];
    put(ws + G_ACC + wg * 512 + tid, o * lds[L_SC + (tid >> 7)], tag);
  }
  STAMP(6);
  {
    const int row = tid & 127, part = tid >> 7;
    u64 av[8];
    for (;;) {
      bool ok = true;
#pragma unroll
      for (int i = 0; i < 8; ++i) av[i] = get(ws + G_ACC + (g * NSPLIT + 8 * part + i) * 512 + hh * DH + row);
#pragma unroll
      for (int i = 0; i < 8; ++i) ok &= okg(av[i], tag);
      if (__all(ok)) break;
      if ((long long)(rt() - tstart) > TCAP) break;
      __builtin_amdgcn_s_sleep(2);
    }
    float o = 0.f;
#pragma unroll
    for (int i = 0; i < 8; ++i) o += valg(av[i]);
    lds[L_H + tid] = o;
  }
  __syncthreads();
  STAMP(7);
  {
    const float oa = rbf((lds[L_H + prow] + lds[L_H + 128 + prow]) + (lds[L_H + 256 + prow] + lds[L_H + 384 + prow]));
    const float ob = rbf((lds[L_H + prow + 1] + lds[L_H + 128 + prow + 1]) + (lds[L_H + 256 + prow + 1] + lds[L_H + 384 + prow + 1]));
    float a[8];
#pragma unroll
    for (int i = 0; i < 4; ++i) {
      a[2 * i] = oa * lo16(woa[i]) + ob * lo16(wob[i]);
      a[2 * i + 1] = oa * hi16(woa[i]) + ob * hi16(wob[i]);
    }
#pragma unroll
    for (int i = 0; i < 8; ++i) { float t = a[i]; t += dpp<ROR8>(t); a[i] = xrow_sum(t); }
    if (lane < 8) {
      *(f32x4*)(lds + L_RED + w * 64 + 8 * lane) = f32x4{a[0], a[1], a[2], a[3]};
      *(f32x4*)(lds + L_RED + w * 64 + 8 * lane + 4) = f32x4{a[4], a[5], a[6], a[7]};
    }
  }
  __syncthreads();
  if (tid < 64) {
    float t = 0.f;
#pragma unroll
    for (int i = 0; i < NWAVE; ++i) t += lds[L_RED + i * 64 + tid];
    put(ws + G_Z + (4 * g + hh) * 512 + cb + tid, t, tag);
  }
  STAMP(8);
  {
    u64 zv[32];
    unsigned pend = 0xffu;
    for (;;) {
#pragma unroll
      for (int gg = 0; gg < 8; ++gg)
        if (pend & (1u << gg)) {
#pragma unroll
          for (int i = 0; i < 4; ++i) zv[4 * gg + i] = get(ws + G_Z + (4 * gg + i) * 512 + tid);
        }
#pragma unroll
      for (int gg = 0; gg < 8; ++gg) {
        bool ok = true;
#pragma unroll
        for (int i = 0; i < 4; ++i) ok &= okg(zv[4 * gg + i], tag);
        if (__all(ok)) pend &= ~(1u << gg);
      }
      if (pend == 0) break;
      if ((long long)(rt() - tstart) > TCAP) break;
      __builtin_amdgcn_s_sleep(2);
    }
    float t = 0.f;
#pragma unroll
    for (int i = 0; i < 32; ++i) t += valg(zv[i]);
    lds[L_PART + tid] = rbf(bf2f(xt16) + rbf(t));
  }
  __syncthreads();
  STAMP(9);
  {
    float v[8];
    const f32x4 ha = *(const f32x4*)(lds + L_PART + 8 * lane), hb = *(const f32x4*)(lds + L_PART + 8 * lane + 4);
#pragma unroll
    for (int i = 0; i < 4; ++i) { v[i] = ha[i]; v[4 + i] = hb[i]; }
    float mb1, sd1, mb2, sd2;
    ln_stats(v, mb1, sd1);
#pragma unroll
    for (int i = 0; i < 8; ++i) v[i] = ln_apply(v[i], mb1, sd1);
    ln_stats(v, mb2, sd2);
    const int c0 = 2 * blockIdx.x;
    const float h0 = lds[L_PART + c0], h1 = lds[L_PART + c0 + 1];
    const float n0 = ln_apply(ln_apply(h0, mb1, sd1), mb2, sd2), n1 = ln_apply(ln_apply(h1, mb1, sd1), mb2, sd2);
    float y0 = 0.f, y1 = 0.f;
    if (tid < FF / 4) {
#pragma unroll
      for (int i = 0; i < 4; ++i) {
        const float a1 = (i & 1) ? hi16(w1v[i >> 1]) : lo16(w1v[i >> 1]);
        const float bb = (i & 1) ? hi16(b1v[i >> 1]) : lo16(b1v[i >> 1]);
        const float a2 = (i & 1) ? hi16(w2v[i >> 1]) : lo16(w2v[i >> 1]);
        y0 += rbf(gelu_tanh(rbf(rbf(n0 * a1) + bb))) * a2;
        y1 += rbf(gelu_tanh(rbf(rbf(n1 * a1) + bb))) * a2;
      }
    }
    y0 = wave_sum(y0); y1 = wave_sum(y1);
    if (lane == 0) { lds[L_RED + 2 * w] = y0; lds[L_RED + 2 * w + 1] = y1; }
    __syncthreads();
    if (tid == 0) {
      float s0 = 0.f, s1 = 0.f;
#pragma unroll
      for (int i = 0; i < NWAVE; ++i) { s0 += lds[L_RED + 2 * i]; s1 += lds[L_RED + 2 * i + 1]; }
      const float bb2 = bf2f(b216);
      *(unsigned*)(out + c0) = cvtpk(h0 + rbf(rbf(s0) + bb2), h1 + rbf(rbf(s1) + bb2));
      if (blockIdx.x == 0) __hip_atomic_store(ws + G_EPOCH, mixv, RLX_AGENT);
    }
  }
  STAMP(10);
}

extern "C" void kernel_launch(void* const* d_in, const int* in_sizes, int n_in, void* d_out, int out_size, void* d_ws, size_t ws_size,
                              hipStream_t stream) {
  k_fused<<<NWG, NTHR, 0, stream>>>((const u16*)d_in[0], (const u16*)d_in[1], (const u16*)d_in[2], (const u16*)d_in[3], (const u16*)d_in[4],
                                    (const u16*)d_in[6], (const u16*)d_in[7], (const u16*)d_in[8], (const u16*)d_in[9], (u16*)d_out, (u64*)d_ws);
}
```

```cpp
#include <hip/hip_runtime.h>
#include <hip/hip_bf16.h>
#include <cstdint>
#include <type_traits>

#define W0 32
#define WV 32
#define THR 40
#define TILES_EVEN 68
#define SLPL 32
#define SLPQ 0
#define SLPZ 0
#define ORDB 0
#define GATE1 5
#define MANY1 8
#define MANY2 12
#define SLP1 1
#define SLP2 1
#define CODEPF 0
#define PAT 0
#define UNTHR 1
#define DIAG 0

typedef unsigned v4u __attribute__((ext_vector_type(4)));
typedef unsigned v2u __attribute__((ext_vector_type(2)));
typedef float f32x4 __attribute__((ext_vector_type(4)));
typedef float f32x2 __attribute__((ext_vector_type(2)));
typedef __bf16 bf16x8 __attribute__((ext_vector_type(8)));
typedef __bf16 bf16x2 __attribute__((ext_vector_type(2)));
typedef unsigned short u16;
typedef unsigned long long u64;
typedef __amdgpu_buffer_rsrc_t rsrc_t;
#define GAS __attribute__((address_space(1)))
#define LAS __attribute__((address_space(3)))
typedef GAS u64 gu64;
#define RLX_AGENT __ATOMIC_RELAXED, __HIP_MEMORY_SCOPE_AGENT

constexpr int D = 512, SK = 65536, DH = 128, FF = 1792;
constexpr int NSPLIT = 32, NWAVE = 8, NTHR = 512, NWG = 256;
constexpr int TE = TILES_EVEN, TO = 128 - TE;
constexpr int NTILE = (TE + 7) / 8;
constexpr int NLK = NTILE * 8;
constexpr int NL = 2 * NLK;
constexpr float EPS = 1e-5f;
constexpr float SCALE = 0.08838834764831845f;
constexpr float LOG2E = 1.4426950408889634f;
constexpr int SPIN_CAP = 1 << 19;

constexpr int G_EPOCH = 0;
constexpr int G_Q = 512;
constexpr int G_ML = G_Q + 8 * 4 * 512;
constexpr int G_Z = G_ML + 8 * 4 * 32 * 2;
constexpr int G_ACC = G_Z + 16 * 512;
constexpr int G_END = G_ACC + 256 * 512;
constexpr int G_DIAG = G_END;

__device__ __forceinline__ float bf2f(u16 b) { return __uint_as_float((unsigned)b << 16); }
__device__ __forceinline__ float rbf(float x) { return (float)(__bf16)x; }
__device__ __forceinline__ float asf(unsigned w) { return __uint_as_float(w); }
__device__ __forceinline__ unsigned cvtpk(float lo, float hi) {
  bf16x2 r = {(__bf16)lo, (__bf16)hi};
  return __builtin_bit_cast(unsigned, r);
}
__device__ __forceinline__ f32x2 unpk(unsigned w) { return f32x2{asf(w << 16), asf(w & 0xffff0000u)}; }
__device__ __forceinline__ f32x2 rbf2(f32x2 v) {
  const unsigned p = cvtpk(v[0], v[1]);
  return f32x2{asf(__builtin_amdgcn_perm(p, p, 0x01000c0cu)), asf(__builtin_amdgcn_perm(p, p, 0x03020c0cu))};
}
__device__ __forceinline__ float dot2(unsigned a, unsigned b, float c) {
  return __builtin_amdgcn_fdot2_f32_bf16(__builtin_bit_cast(bf16x2, a), __builtin_bit_cast(bf16x2, b), c, false);
}
__device__ __forceinline__ f32x4 mfma(v4u a, v4u b, f32x4 c) {
  return __builtin_amdgcn_mfma_f32_16x16x32_bf16(__builtin_bit_cast(bf16x8, a), __builtin_bit_cast(bf16x8, b), c, 0, 0, 0);
}
template <int CTRL> __device__ __forceinline__ float dpp(float x) {
  return __builtin_bit_cast(float, __builtin_amdgcn_mov_dpp(__builtin_bit_cast(int, x), CTRL, 0xf, 0xf, true));
}
template <int CTRL, int RM> __device__ __forceinline__ float dppz(float x) {
  return __builtin_bit_cast(float, __builtin_amdgcn_update_dpp(0, __builtin_bit_cast(int, x), CTRL, RM, 0xf, false));
}
constexpr int XOR1 = 0xB1, XOR2 = 0x4E, ROR8 = 0x128, ROR4 = 0x124, ROR12 = 0x12C, HMIR = 0x141, BC15 = 0x142, BC31 = 0x143;
__device__ __forceinline__ float swap32_add(float a, float b) {
  auto s = __builtin_amdgcn_permlane32_swap(__float_as_uint(a), __float_as_uint(b), false, false);
  return asf(s[0]) + asf(s[1]);
}
__device__ __forceinline__ float swap16_add(float a, float b) {
  auto s = __builtin_amdgcn_permlane16_swap(__float_as_uint(a), __float_as_uint(b), false, false);
  return asf(s[0]) + asf(s[1]);
}
__device__ __forceinline__ float swap16_max(float x) {
  auto s = __builtin_amdgcn_permlane16_swap(__float_as_uint(x), __float_as_uint(x), false, false);
  return fmaxf(asf(s[0]), asf(s[1]));
}
__device__ __forceinline__ float xrow_sum(float x) { x = swap16_add(x, x); return swap32_add(x, x); }
__device__ __forceinline__ float xrow_max(float x) {
  x = swap16_max(x);
  auto t = __builtin_amdgcn_permlane32_swap(__float_as_uint(x), __float_as_uint(x), false, false);
  return fmaxf(asf(t[0]), asf(t[1]));
}
__device__ __forceinline__ float rdlane(float x, int l) { return __builtin_bit_cast(float, __builtin_amdgcn_readlane(__builtin_bit_cast(int, x), l)); }
__device__ __forceinline__ void wave_sum2_l63(float& a, float& b) {
  a += dpp<XOR1>(a); b += dpp<XOR1>(b);
  a += dpp<XOR2>(a); b += dpp<XOR2>(b);
  a += dpp<ROR4>(a); b += dpp<ROR4>(b);
  a += dpp<ROR8>(a); b += dpp<ROR8>(b);
  a += dppz<BC15, 0xa>(a); b += dppz<BC15, 0xa>(b);
  a += dppz<BC31, 0xc>(a); b += dppz<BC31, 0xc>(b);
}
__device__ __forceinline__ void ln_finish(float s1, float s2, float c, float& mb, float& rs) {
  wave_sum2_l63(s1, s2);
  const float dm = rdlane(s1, 63) * (1.f / D);
  const float var = fmaxf(rdlane(s2, 63) * (1.f / D) - dm * dm, 0.f);
  const f32x2 mv = rbf2(f32x2{c + dm, var});
  mb = mv[0];
  rs = __builtin_amdgcn_rcpf(rbf(__builtin_amdgcn_sqrtf(rbf(mv[1] + EPS))));
}
__device__ __forceinline__ void ln_stats(const f32x2 (&v)[4], float& mb, float& rs) {
  const float c = rdlane(v[0][0], 0);
  const f32x2 d0 = v[0] - c, d1 = v[1] - c, d2 = v[2] - c, d3 = v[3] - c;
  const f32x2 s1 = (d0 + d1) + (d2 + d3), s2 = (d0 * d0 + d1 * d1) + (d2 * d2 + d3 * d3);
  ln_finish(s1[0] + s1[1], s2[0] + s2[1], c, mb, rs);
}
__device__ __forceinline__ unsigned ln_apply_pk(f32x2 v, float mb, float rs) { const f32x2 t = rbf2(v - mb) * rs; return cvtpk(t[0], t[1]); }

constexpr int AUX_SC1 = 16, AUX_NT = 2;
__device__ __forceinline__ v4u get2(rsrc_t rs, int voff, int soff) { return __builtin_amdgcn_raw_buffer_load_b128(rs, voff, soff, AUX_SC1); }
__device__ __forceinline__ void put2(rsrc_t rs, int voff, int soff, float a, float b, unsigned tag) {
  __builtin_amdgcn_raw_buffer_store_b128(v4u{tag, __float_as_uint(a), tag, __float_as_uint(b)}, rs, voff + soff, 0, AUX_SC1);
}
__device__ __forceinline__ unsigned stale(v4u g, unsigned tag) { return (g[0] ^ tag) | (g[2] ^ tag); }
__device__ __forceinline__ bool any(unsigned bad) {
  asm("" : "+v"(bad));
  return __builtin_amdgcn_ballot_w64(bad != 0u) != 0ull;
}
template <int I, int N> struct Loop {
  template <class F> static __device__ __forceinline__ void run(F&& f) {
    if constexpr (I < N) { f(std::integral_constant<int, I>{}); Loop<I + 1, N>::run(f); }
  }
};
template <int N, class LD, class NAP> __device__ __forceinline__ void gather(v4u (&g)[N], unsigned tag, LD&& ld, NAP&& nap) {
  unsigned pend = (1u << N) - 1u;
#pragma unroll 1
  for (int spin = 0; spin < SPIN_CAP; ++spin) {
    asm volatile("" ::: "memory");
    Loop<0, N>::run([&](auto I) { if (pend & (1u << I.value)) g[I.value] = ld(I); });
    Loop<0, N>::run([&](auto I) { if (pend & (1u << I.value)) { if (!any(stale(g[I.value], tag))) pend &= ~(1u << I.value); } });
    if (pend == 0u) break;
    nap(pend);
  }
}
#define G16_LD(S, J) ".set gi_, " #J "\n\t.rept 4\n\tbuffer_load_dwordx4 v[128+4*gi_:131+4*gi_], %[vo], %[rs], " S " offen offset:(gi_&3)*1024 sc1\n\t.set gi_, gi_+1\n\t.endr\n\t"
#define G16_RE(S, J) ".set gi_, " #J "\n\t.rept 4\n\tv_cmpx_ne_u32_e32 vcc, 0, v[108+gi_]\n\tbuffer_load_dwordx4 v[128+4*gi_:131+4*gi_], %[vo], %[rs], " S " offen offset:(gi_&3)*1024 sc1\n\ts_mov_b64 exec, -1\n\t.set gi_, gi_+1\n\t.endr\n\t"
template <int SLP> __device__ __forceinline__ void gather16(v4u (&g)[16], rsrc_t rs, int voff, int soff, unsigned tag) {
  unsigned spin = SPIN_CAP;
  const int s0 = __builtin_amdgcn_readfirstlane(soff);
  asm volatile(
      "s_nop 4\n\t"
      G16_LD("%[s0]", 0) G16_LD("%[s1]", 4) G16_LD("%[s2]", 8) G16_LD("%[s3]", 12)
      "1:\n\t"
      "s_waitcnt vmcnt(0)\n\t"
      ".set gi_, 0\n\t.rept 16\n\tv_bitop3_b32 v[108+gi_], v[128+4*gi_], v[130+4*gi_], %[tg] bitop3:0x7e\n\t.set gi_, gi_+1\n\t.endr\n\t"
      "v_or3_b32 v124, v108, v109, v110\n\t"
      ".set gi_, 3\n\t.rept 6\n\tv_or3_b32 v124, v124, v[108+gi_], v[109+gi_]\n\t.set gi_, gi_+2\n\t.endr\n\t"
      "v_or_b32_e32 v124, v124, v123\n\t"
      "v_cmp_ne_u32_e32 vcc, 0, v124\n\t"
      "s_cbranch_vccz 2f\n\t"
      "s_sub_u32 %[sp], %[sp], 1\n\t"
      "s_cbranch_scc1 2f\n\t"
      "s_sleep %[sl]\n\t"
      G16_RE("%[s0]", 0) G16_RE("%[s1]", 4) G16_RE("%[s2]", 8) G16_RE("%[s3]", 12)
      "s_branch 1b\n\t"
      "2:"
      : "={v[128:131]}"(g[0]), "={v[132:135]}"(g[1]), "={v[136:139]}"(g[2]), "={v[140:143]}"(g[3]), "={v[144:147]}"(g[4]), "={v[148:151]}"(g[5]),
        "={v[152:155]}"(g[6]), "={v[156:159]}"(g[7]), "={v[160:163]}"(g[8]), "={v[164:167]}"(g[9]), "={v[168:171]}"(g[10]), "={v[172:175]}"(g[11]),
        "={v[176:179]}"(g[12]), "={v[180:183]}"(g[13]), "={v[184:187]}"(g[14]), "={v[188:191]}"(g[15]), [sp] "+s"(spin)
      : [vo] "{v125}"(voff), [rs] "s"(rs), [s0] "s"(s0), [s1] "s"(s0 + 4096), [s2] "s"(s0 + 8192), [s3] "s"(s0 + 12288), [tg] "s"(tag), [sl] "i"(SLP)
      : "v108", "v109", "v110", "v111", "v112", "v113", "v114", "v115", "v116", "v117", "v118", "v119", "v120", "v121", "v122", "v123", "v124",
        "vcc", "scc", "memory");
}
__device__ __forceinline__ u64 rt() { return __builtin_amdgcn_s_memrealtime(); }

constexpr int BLK_STRIDE = 4096;
template <int I> __device__ __forceinline__ v4u ldkv(rsrc_t rk, rsrc_t rv, int koff, int voff) {
  if constexpr (I < NLK) return __builtin_amdgcn_raw_buffer_load_b128(rk, koff + (I & 3) * 1024, (I >> 2) * BLK_STRIDE, AUX_NT);
  else return __builtin_amdgcn_raw_buffer_load_b128(rv, voff + ((I - NLK) & 3) * 1024, ((I - NLK) >> 2) * BLK_STRIDE, AUX_NT);
}
template <int BM> __device__ __forceinline__ float bsel(float old, float src) {
  return __builtin_bit_cast(float, __builtin_amdgcn_update_dpp(__builtin_bit_cast(int, old), __builtin_bit_cast(int, src), 0xE4, 0xf, BM, false));
}
#define STR2(x) #x
#define STR(x) STR2(x)
#define WSTAMP(k) do { } while (0)
#define STAMP(k) do { } while (0)
#define STAMPW(k, ww) do { } while (0)
#define NTL(p) __builtin_nontemporal_load(p)

constexpr int L_PART = 0;
constexpr int L_RED = L_PART + 8 * 512;
constexpr int L_WT = L_RED + 8 * 64;
constexpr int L_OB = L_WT + 4 * 32;
constexpr int L_MX = L_WT + 8 * 32;
constexpr int L_LS = L_MX + 32;
constexpr int L_SC = L_LS + 32;
constexpr int L_CNT = L_SC + 32;
constexpr int L_Q = L_CNT + 8;
constexpr int L_H = L_Q + 256;
constexpr int L_END = L_H + 512;

__global__ void __launch_bounds__(NTHR, 2) k_fused(const u16* __restrict__ x, const u16* __restrict__ kc, const u16* __restrict__ vc,
                                                   const u16* __restrict__ wq, u64* __restrict__ wsp, const u16* __restrict__ wo,
                                                   const u16* __restrict__ w1, const u16* __restrict__ b1, const u16* __restrict__ w2,
                                                   const u16* __restrict__ b2, u16* __restrict__ out) {
  __shared__ __attribute__((aligned(16))) float lds[L_END];
  gu64* ws = (gu64*)wsp;
  const int tid = threadIdx.x, lane = tid & 63, w = __builtin_amdgcn_readfirstlane(tid >> 6);
  const int par = blockIdx.x & 1, jx = blockIdx.x >> 3;
  const int g = (blockIdx.x & 6) + (jx >> 4), s = 16 * par + (jx & 15);
  STAMP(0);
  volatile LAS unsigned* cnt = (volatile LAS unsigned*)(lds + L_CNT);
  if (tid < 4) cnt[tid] = 0u;
  asm volatile("s_waitcnt lgkmcnt(0)\n\ts_barrier" :: "s"(x), "s"(kc), "s"(vc), "s"(wq), "s"(wsp) : "memory");
  unsigned tag;
  const rsrc_t rw = __builtin_amdgcn_make_buffer_rsrc((void*)wsp, 0, G_END * 8, 0x00020000);
  const int ntw = par ? TO : TE, tlo = (ntw * w) >> 3, nt = ((ntw * (w + 1)) >> 3) - tlo;
  const size_t key0 = (size_t)g * SK + 32 * ((par ? 16 * TE : 0) + (jx & 15) * ntw + tlo);
  const int lg = lane >> 4, bank = (lane >> 2) & 3, lh = lane & 3;
  const rsrc_t rk = __builtin_amdgcn_make_buffer_rsrc((void*)(kc + key0 * DH), 0, nt * 8192, 0x00020000);
  const rsrc_t rv = __builtin_amdgcn_make_buffer_rsrc((void*)(vc + key0 * DH), 0, nt * 8192, 0x00020000);
  const int koff = 16 * (16 * bank + 4 * lg + lh), voff = 16 * lane;
  v4u buf[NL];
  if (__builtin_expect(w == 0, 1)) {
    __builtin_amdgcn_s_setprio(3);
    const int kq = s >> 3, cq = 64 * (s & 7);
    const rsrc_t rq = __builtin_amdgcn_make_buffer_rsrc((void*)(wq + (size_t)kq * 128 * 4096 + 512 * g + cq), 0, 127 * 8192 + 128, 0x00020000);
    const v4u xv = NTL((const v4u*)(x + 8 * lane));
    const v4u xa = NTL((const v4u*)(x + 128 * kq + 16 * (lane >> 3))), xb = NTL((const v4u*)(x + 128 * kq + 16 * (lane >> 3) + 8));
    v4u wv[16];
    const int qoff = (lane >> 3) * (16 * 8192) + (lane & 7) * 16;
#pragma unroll
    for (int i = 0; i < 16; ++i) wv[i] = __builtin_amdgcn_raw_buffer_load_b128(rq, qoff, i * 8192, AUX_NT);
    const u64 epoch = *(const __attribute__((address_space(4))) u64*)(wsp + G_EPOCH);
    __builtin_amdgcn_sched_barrier(0);
    if (ORDB) asm volatile("s_barrier" ::: "memory");
    f32x2 v[4];
#pragma unroll
    for (int i = 0; i < 4; ++i) v[i] = unpk(xv[i]);
    float mb, rs;
    ln_stats(v, mb, rs);
    unsigned xn[8];
#pragma unroll
    for (int i = 0; i < 8; ++i) xn[i] = ln_apply_pk(unpk(i < 4 ? xa[i & 3] : xb[i & 3]), mb, rs);
    {
      u64 mixv = epoch + 0x9E3779B97F4A7C15ull;
      mixv = (mixv ^ (mixv >> 30)) * 0xBF58476D1CE4E5B9ull;
      mixv = (mixv ^ (mixv >> 27)) * 0x94D049BB133111EBull;
      mixv ^= mixv >> 31;
      const unsigned lo = (unsigned)mixv;
      tag = __builtin_amdgcn_readfirstlane(lo == (unsigned)epoch ? lo ^ 1u : lo);
      cnt[4] = tag;
      cnt[5] = (unsigned)(mixv >> 32);
    }
    float a[8] = {0.f, 0.f, 0.f, 0.f, 0.f, 0.f, 0.f, 0.f};
#pragma unroll
    for (int j = 0; j < 8; ++j)
#pragma unroll
      for (int d = 0; d < 4; ++d) {
        a[2 * d] = dot2(__builtin_amdgcn_perm(wv[2 * j + 1][d], wv[2 * j][d], 0x05040100u), xn[j], a[2 * d]);
        a[2 * d + 1] = dot2(__builtin_amdgcn_perm(wv[2 * j + 1][d], wv[2 * j][d], 0x07060302u), xn[j], a[2 * d + 1]);
      }
    float c0 = swap16_add(swap32_add(a[0], a[4]), swap32_add(a[2], a[6])), c1 = swap16_add(swap32_add(a[1], a[5]), swap32_add(a[3], a[7]));
    c0 += dpp<ROR8>(c0); c1 += dpp<ROR8>(c1);
    if ((lane & 8) == 0) put2(rw, (lane & 7) * 64 + (lane >> 4) * 16, (G_Q + (g * 4 + kq) * 512 + cq) * 8, c0, c1, tag);
    STAMP(1);
    v4u qv[16];
    gather16<SLPQ>(qv, rw, voff, (G_Q + g * 2048) * 8, tag);
#pragma unroll
    for (int i = 0; i < 4; ++i)
      ((unsigned*)(lds + L_Q))[64 * i + lane] = cvtpk((asf(qv[i][1]) + asf(qv[4 + i][1])) + (asf(qv[8 + i][1]) + asf(qv[12 + i][1])),
                                                      (asf(qv[i][3]) + asf(qv[4 + i][3])) + (asf(qv[8 + i][3]) + asf(qv[12 + i][3])));
    asm volatile("s_waitcnt lgkmcnt(0)" ::: "memory");
    cnt[1] = 1u;
    __builtin_amdgcn_s_setprio(0);
    STAMP(2);
    WSTAMP(0);
    Loop<0, W0>::run([&](auto I) { buf[I.value] = ldkv<I.value>(rk, rv, koff, voff); });
  } else {
    if (ORDB) asm volatile("s_barrier" ::: "memory");
    Loop<0, W0>::run([&](auto I) { buf[I.value] = ldkv<I.value>(rk, rv, koff, voff); });
    WSTAMP(0);
#pragma unroll 1
    for (int spin = 0; spin < (SPIN_CAP << 4) && __builtin_amdgcn_readfirstlane(cnt[1]) == 0u; ++spin) __builtin_amdgcn_s_sleep(2);
    asm volatile("" ::: "memory");
    tag = __builtin_amdgcn_readfirstlane(cnt[4]);
  }
  STAMPW(3, 1);
  const v4u qb = *(const v4u*)((const u16*)(lds + L_Q) + lh * DH + 8 * (4 * lg + bank));
  __builtin_amdgcn_sched_barrier(0);

  float X[NLK / 4];
  Loop<0, NLK / 4>::run([&](auto T) {
    constexpr int t = T.value;
    f32x4 d[4];
    Loop<0, 4>::run([&](auto M) {
      constexpr int i = 4 * t + M.value;
      d[M.value] = mfma(buf[i], qb, f32x4{0.f, 0.f, 0.f, 0.f});
      if constexpr (i + W0 < NLK + WV) buf[i + W0] = ldkv<i + W0>(rk, rv, koff, voff);
    });
    float R[4];
#pragma unroll
    for (int k = 0; k < 4; ++k) R[k] = bsel<8>(bsel<4>(bsel<2>(d[k][0], d[(k + 1) & 3][1]), d[(k + 2) & 3][2]), d[(k + 3) & 3][3]);
    X[t] = ((R[0] + dpp<ROR4>(R[1])) + dpp<ROR8>(R[2])) + dpp<ROR12>(R[3]);
    asm volatile("" : "+v"(X[t]));
    __builtin_amdgcn_sched_barrier(0);
  });
  STAMP(4);
  WSTAMP(1);
  unsigned PK[NLK / 8];
  {
    float m = -INFINITY;
#pragma unroll
    for (int t = 0; t < NLK / 8; ++t) {
      f32x2 sc = rbf2(rbf2(f32x2{X[2 * t], X[2 * t + 1]}) * SCALE);
      if (t >= TO / 8 && t >= nt) sc = f32x2{-INFINITY, -INFINITY};
      X[2 * t] = sc[0]; X[2 * t + 1] = sc[1];
      m = fmaxf(m, fmaxf(sc[0], sc[1]));
    }
    m = fmaxf(m, dpp<ROR4>(m)); m = fmaxf(m, dpp<ROR8>(m)); m = xrow_max(m);
    float l = 0.f;
#pragma unroll
    for (int t = 0; t < NLK / 4; ++t) { X[t] = __builtin_amdgcn_exp2f((X[t] - m) * LOG2E); l += X[t]; }
    l += dpp<ROR4>(l); l += dpp<ROR8>(l); l = xrow_sum(l);
#pragma unroll
    for (int t = 0; t < NLK / 8; ++t) PK[t] = cvtpk(X[2 * t], X[2 * t + 1]);
    unsigned old = 0u;
    if (lane < 4) { lds[L_MX + w * 4 + lane] = m; lds[L_LS + w * 4 + lane] = l; }
    if (lane == 0) old = __hip_atomic_fetch_add((unsigned*)(lds + L_CNT), 1u, __ATOMIC_ACQ_REL, __HIP_MEMORY_SCOPE_WORKGROUP);
    if (__builtin_amdgcn_readfirstlane(old) == NWAVE - 1) {
      const float mw = lds[L_MX + (lane & 31)], lw = lds[L_LS + (lane & 31)];
      float M = fmaxf(mw, dpp<ROR4>(mw)); M = fmaxf(M, dpp<ROR8>(M)); M = swap16_max(M);
      const float sc = __builtin_amdgcn_exp2f((mw - M) * LOG2E);
      float L = lw * sc;
      L += dpp<ROR4>(L); L += dpp<ROR8>(L); L = swap16_add(L, L);
      if (lane < 32) lds[L_SC + lh * 8 + (lane >> 2)] = sc;
      if (lane < 4) put2(rw, lane * 512, (G_ML + g * 256 + s * 2) * 8, M, L, tag);
    }
  }
  const int hp = s & 1, cbk = (s >> 1) & 7;
  const rsrc_t ro = __builtin_amdgcn_make_buffer_rsrc((void*)(wo + (size_t)(512 * g + 256 * hp + 64 * (w & 3)) * D + 64 * cbk), 0, par ? 0 : 63 * 1024 + 128, 0x00020000);
  v4u wov[8], w1v, b1v, w2v;
  const unsigned xt = NTL((const unsigned*)(x + 128 * (w & 3) + 2 * lane));
  const u16 b216 = NTL(b2);

  unsigned bm[4];
#pragma unroll
  for (int mm = 0; mm < 4; ++mm) bm[mm] = bank == mm ? 0xffffffffu : 0u;
  f32x4 acc[8];
#pragma unroll
  for (int j = 0; j < 8; ++j) acc[j] = f32x4{0.f, 0.f, 0.f, 0.f};
  __builtin_amdgcn_sched_barrier(0);
  Loop<0, NLK / 8>::run([&](auto TT) {
    constexpr int T = TT.value;
    constexpr int base = NLK + 8 * T;
    v4u pa;
#pragma unroll
    for (int mm = 0; mm < 4; ++mm) pa[mm] = PK[T] & bm[mm];
#pragma unroll
    for (int jj = 0; jj < 4; ++jj) {
      v4u b0, b1;
#pragma unroll
      for (int mm = 0; mm < 4; ++mm) {
        b0[mm] = __builtin_amdgcn_perm(buf[base + 4 + mm][jj], buf[base + mm][jj], 0x05040100u);
        b1[mm] = __builtin_amdgcn_perm(buf[base + 4 + mm][jj], buf[base + mm][jj], 0x07060302u);
      }
      acc[2 * jj] = mfma(pa, b0, acc[2 * jj]);
      acc[2 * jj + 1] = mfma(pa, b1, acc[2 * jj + 1]);
    }
    __builtin_amdgcn_sched_barrier(0);
    Loop<0, 8>::run([&](auto I) {
      constexpr int idx = base + I.value;
      if constexpr (idx + WV < NL) buf[idx + WV] = ldkv<idx + WV>(rk, rv, koff, voff);
    });
    if constexpr (base + 8 + WV == NL + 8) {
      if (w < 4) {
#pragma unroll
        for (int i = 0; i < 8; ++i) wov[i] = __builtin_amdgcn_raw_buffer_load_b128(ro, (lane >> 3) * 8192 + (lane & 7) * 16, i * 1024, AUX_NT);
        const int fi = tid < FF / 8 ? tid : 0;
        w1v = NTL((const v4u*)(w1 + 8 * fi)); b1v = NTL((const v4u*)(b1 + 8 * fi)); w2v = NTL((const v4u*)(w2 + 8 * fi));
      }
    }
    __builtin_amdgcn_sched_barrier(0);
  });
  STAMP(5);
  WSTAMP(2);
  STAMPW(11, 7);
  {
    float r2[8];
#pragma unroll
    for (int j = 0; j < 8; ++j) r2[j] = swap16_add(swap32_add(acc[j][0], acc[j][2]), swap32_add(acc[j][1], acc[j][3]));
    *(f32x4*)(lds + L_PART + w * 512 + lane * 8) = f32x4{r2[0], r2[1], r2[2], r2[3]};
    *(f32x4*)(lds + L_PART + w * 512 + lane * 8 + 4) = f32x4{r2[4], r2[5], r2[6], r2[7]};
  }
  if (lane == 0) __hip_atomic_fetch_add((unsigned*)(lds + L_CNT) + 2, 1u, __ATOMIC_RELAXED, __HIP_MEMORY_SCOPE_WORKGROUP);
  f32x2 w1f[4], b1f[4];
  v4u wb[8];
  f32x4 wt[4];
  bool have_wt = false;
  auto mld = [&](auto) { return get2(rw, (lane & 31) * 16, (G_ML + (g * 4 + 2 * hp + ((w >> 1) & 1)) * 64) * 8); };
  auto weights = [&](v4u mv) {
    const float mp = asf(mv[1]);
    float M = fmaxf(mp, dpp<XOR1>(mp)); M = fmaxf(M, dpp<XOR2>(M)); M = fmaxf(M, dpp<ROR4>(M)); M = fmaxf(M, dpp<ROR8>(M)); M = swap16_max(M);
    const float e = __builtin_amdgcn_exp2f((mp - M) * LOG2E);
    float L = e * asf(mv[3]);
    L += dpp<XOR1>(L); L += dpp<XOR2>(L); L += dpp<ROR4>(L); L += dpp<ROR8>(L); L = swap16_add(L, L);
    lds[L_WT + w * 32 + (lane & 31)] = e * __builtin_amdgcn_rcpf(L);
#pragma unroll
    for (int i = 0; i < 4; ++i) wt[i] = *(const f32x4*)(lds + L_WT + w * 32 + 16 * (lane >> 5) + 4 * i);
#pragma unroll
    for (int i = 0; i < 4; ++i) asm volatile("" : "+v"(wt[i]));
  };
  if (w < 4) {
#pragma unroll
    for (int i = 0; i < 4; ++i) { w1f[i] = unpk(w1v[i]); b1f[i] = unpk(b1v[i]); }
    if (tid >= FF / 8) w2v = v4u{0u, 0u, 0u, 0u};
#pragma unroll
    for (int i = 0; i < 4; ++i) asm volatile("" : "+v"(w1f[i]), "+v"(b1f[i]));
    asm volatile("" : "+v"(w2v));
    if (par == 0) {
#pragma unroll
      for (int jj = 0; jj < 4; ++jj)
#pragma unroll
        for (int mm = 0; mm < 4; ++mm) {
          wb[2 * jj][mm] = __builtin_amdgcn_perm(wov[2 * mm + 1][jj], wov[2 * mm][jj], 0x05040100u);
          wb[2 * jj + 1][mm] = __builtin_amdgcn_perm(wov[2 * mm + 1][jj], wov[2 * mm][jj], 0x07060302u);
        }
#pragma unroll
      for (int i = 0; i < 8; ++i) asm volatile("" : "+v"(wb[i]));
      unsigned done = 0u;
#pragma unroll 1
      for (int spin = 0; spin < (SPIN_CAP << 4) && (done = __builtin_amdgcn_readfirstlane(cnt[2])) < GATE1; ++spin) __builtin_amdgcn_s_sleep(8);
      if (done < NWAVE - 1) {
        const v4u mv = mld(0);
        if (!any(stale(mv, tag))) { weights(mv); have_wt = true; }
      }
    }
  }
  WSTAMP(3);
  __syncthreads();
  if (w < 4) {
    const f32x4 sa = *(const f32x4*)(lds + L_SC + w * 8), sb = *(const f32x4*)(lds + L_SC + w * 8 + 4);
    f32x2 o0 = *(const f32x2*)(lds + L_PART + 2 * tid) * sa[0], o1 = *(const f32x2*)(lds + L_PART + 4 * 512 + 2 * tid) * sb[0];
#pragma unroll
    for (int i = 1; i < 4; ++i) {
      o0 += *(const f32x2*)(lds + L_PART + i * 512 + 2 * tid) * sa[i];
      o1 += *(const f32x2*)(lds + L_PART + (4 + i) * 512 + 2 * tid) * sb[i];
    }
    o0 += o1;
    put2(rw, (tid >> 5) * 16384 + (tid & 31) * 16, (G_ACC + g * 32 * 512 + (2 * (s & 15) + par) * 64) * 8, o0[0], o0[1], tag);
  }
  STAMP(6);
  if (par == 0) {
    __builtin_amdgcn_s_barrier();
    if (w < 4) {
      if (!have_wt) {
        v4u mv[1];
        gather<1>(mv, tag, mld, [](unsigned) { __builtin_amdgcn_s_sleep(8); });
        weights(mv[0]);
      }
      v4u av[16];
      gather16<SLP1>(av, rw, voff, (G_ACC + (g * 8 + hp * 4 + w) * 2048) * 8, tag);
      float oa[2] = {0.f, 0.f}, ob[2] = {0.f, 0.f};
#pragma unroll
      for (int i = 0; i < 16; ++i) { oa[i & 1] = fmaf(asf(av[i][1]), wt[i >> 2][i & 3], oa[i & 1]); ob[i & 1] = fmaf(asf(av[i][3]), wt[i >> 2][i & 3], ob[i & 1]); }
      const float o = swap32_add(oa[0] + oa[1], ob[0] + ob[1]);
      u16* obp = (u16*)(lds + L_OB) + 64 * w;
      obp[2 * (lane & 31) + (lane >> 5)] = (u16)cvtpk(o, o);
      STAMP(7);
      const v4u af = *(const v4u*)(obp + 16 * lg + 8 * (lane & 1));
      float z[8];
#pragma unroll
      for (int j = 0; j < 8; ++j) { const f32x4 d = mfma(af, wb[j], f32x4{0.f, 0.f, 0.f, 0.f}); z[j] = bsel<0xC>(d[0], d[1]); }
      if (lane < 16) {
        *(f32x4*)(lds + L_RED + w * 128 + lane * 8) = f32x4{z[0], z[1], z[2], z[3]};
        *(f32x4*)(lds + L_RED + w * 128 + lane * 8 + 4) = f32x4{z[4], z[5], z[6], z[7]};
      }
    }
    __syncthreads();
    if (tid < 32) {
      f32x2 t[8];
#pragma unroll
      for (int i = 0; i < 8; ++i) t[i] = *(const f32x2*)(lds + L_RED + i * 64 + 2 * lane);
      const f32x2 zz = ((t[0] + t[1]) + (t[2] + t[3])) + ((t[4] + t[5]) + (t[6] + t[7]));
      put2(rw, voff, (G_Z + ((cbk >> 1) * 16 + 2 * g + hp) * 128 + (cbk & 1) * 64) * 8, zz[0], zz[1], tag);
    }
  }
  __builtin_amdgcn_s_barrier();
  STAMP(8);
  if (w >= 4) {
    v4u zv[16];
    if (SLPZ && par) __builtin_amdgcn_s_sleep(SLPZ);
    gather16<SLP2>(zv, rw, voff, (G_Z + (w & 3) * 2048) * 8, tag);
    float ta[8], tb[8];
#pragma unroll
    for (int i = 0; i < 8; ++i) { ta[i] = asf(zv[2 * i][1]) + asf(zv[2 * i + 1][1]); tb[i] = asf(zv[2 * i][3]) + asf(zv[2 * i + 1][3]); }
    const f32x2 t = {((ta[0] + ta[1]) + (ta[2] + ta[3])) + ((ta[4] + ta[5]) + (ta[6] + ta[7])), ((tb[0] + tb[1]) + (tb[2] + tb[3])) + ((tb[4] + tb[5]) + (tb[6] + tb[7]))};
    *(f32x2*)(lds + L_H + 128 * (w & 3) + 2 * lane) = rbf2(unpk(xt) + rbf2(t));
  }
  __syncthreads();
  STAMP(9);
  if (w >= 4) return;
  {
    const f32x4 ha = *(const f32x4*)(lds + L_H + 8 * lane), hb = *(const f32x4*)(lds + L_H + 8 * lane + 4);
    const int c0 = 2 * blockIdx.x;
    const f32x2 hc = *(const f32x2*)(lds + L_H + c0);
    const f32x2 v[4] = {f32x2{ha[0], ha[1]}, f32x2{ha[2], ha[3]}, f32x2{hb[0], hb[1]}, f32x2{hb[2], hb[3]}};
    float mb1, rs1, mb2, rs2;
    ln_stats(v, mb1, rs1);
    unsigned y1[4];
#pragma unroll
    for (int i = 0; i < 4; ++i) y1[i] = ln_apply_pk(v[i], mb1, rs1);
    const f32x2 n1 = rbf2(rbf2(hc - mb1) * rs1);
    ln_finish(dot2(y1[0], 0x3f803f80u, dot2(y1[1], 0x3f803f80u, 0.f)) + dot2(y1[2], 0x3f803f80u, dot2(y1[3], 0x3f803f80u, 0.f)),
              dot2(y1[0], y1[0], dot2(y1[1], y1[1], 0.f)) + dot2(y1[2], y1[2], dot2(y1[3], y1[3], 0.f)), 0.f, mb2, rs2);
    const f32x2 n = rbf2(rbf2(n1 - mb2) * rs2);
    constexpr float K1 = -2.f * LOG2E * 0.7978845608028654f, K2 = K1 * 0.044715f;
    float y[2] = {0.f, 0.f};
#pragma unroll
    for (int i = 0; i < 4; ++i)
#pragma unroll
      for (int c = 0; c < 2; ++c) {
        const f32x2 u = rbf2(rbf2(w1f[i] * n[c]) + b1f[i]);
        const f32x2 a = u * (u * u * K2 + K1);
        const f32x2 e = f32x2{__builtin_amdgcn_exp2f(a[0]), __builtin_amdgcn_exp2f(a[1])} + 1.f;
        const f32x2 gl = u * f32x2{__builtin_amdgcn_rcpf(e[0]), __builtin_amdgcn_rcpf(e[1])};
        y[c] = dot2(cvtpk(gl[0], gl[1]), w2v[i], y[c]);
      }
    wave_sum2_l63(y[0], y[1]);
    if (lane == 63) *(f32x2*)(lds + L_RED + 2 * w) = f32x2{y[0], y[1]};
    __syncthreads();
    if (tid == 0) {
      const f32x4 ta = *(const f32x4*)(lds + L_RED), tb = *(const f32x4*)(lds + L_RED + 4);
      const f32x2 ysum = (f32x2{ta[0], ta[1]} + f32x2{ta[2], ta[3]}) + (f32x2{tb[0], tb[1]} + f32x2{tb[2], tb[3]});
      const f32x2 r = hc + rbf2(rbf2(ysum) + bf2f(b216));
      *(unsigned*)(out + c0) = cvtpk(r[0], r[1]);
      if (blockIdx.x == 0) __hip_atomic_store(ws + G_EPOCH, ((u64)cnt[5] << 32) | tag, RLX_AGENT);
    }
  }
  STAMP(10);
}

extern "C" void kernel_launch(void* const* d_in, const int* in_sizes, int n_in, void* d_out, int out_size, void* d_ws, size_t ws_size,
                              hipStream_t stream) {
  k_fused<<<NWG, NTHR, 0, stream>>>((const u16*)d_in[0], (const u16*)d_in[1], (const u16*)d_in[2], (const u16*)d_in[3], (u64*)d_ws, (const u16*)d_in[4],
                                    (const u16*)d_in[6], (const u16*)d_in[7], (const u16*)d_in[8], (const u16*)d_in[9], (u16*)d_out);
}
```

```cpp
#define TILES_EVEN 64
#include <hip/hip_runtime.h>
#include <hip/hip_bf16.h>
#include <cstdint>
#include <type_traits>

#define KAUX 2
#define VAUX 2
#define WAUX 2
#define W0 32
#define WV 32
#define THR 40
#define SLPL 32
#define SLPQ 0
#define SLPZ 0
#define ORDB 0
#define GATE1 5
#define MANY1 8
#define MANY2 12
#define SLP1 1
#define SLP2 1
#define CODEPF 0
#define PAT 0
#define UNTHR 1
#define DIAG 0

typedef unsigned v4u __attribute__((ext_vector_type(4)));
typedef unsigned v2u __attribute__((ext_vector_type(2)));
typedef float f32x4 __attribute__((ext_vector_type(4)));
typedef float f32x2 __attribute__((ext_vector_type(2)));
typedef __bf16 bf16x8 __attribute__((ext_vector_type(8)));
typedef __bf16 bf16x2 __attribute__((ext_vector_type(2)));
typedef unsigned short u16;
typedef unsigned long long u64;
typedef __amdgpu_buffer_rsrc_t rsrc_t;
#define GAS __attribute__((address_space(1)))
#define LAS __attribute__((address_space(3)))
typedef GAS u64 gu64;
#define RLX_AGENT __ATOMIC_RELAXED, __HIP_MEMORY_SCOPE_AGENT

constexpr int D = 512, SK = 65536, DH = 128, FF = 1792;
constexpr int NSPLIT = 32, NWAVE = 8, NTHR = 512, NWG = 256;
constexpr int TE = TILES_EVEN, TO = 128 - TE;
constexpr int NTILE = (TE + 7) / 8;
constexpr int NLK = NTILE * 8;
constexpr int NL = 2 * NLK;
constexpr float EPS = 1e-5f;
constexpr float SCALE = 0.08838834764831845f;
constexpr float LOG2E = 1.4426950408889634f;
constexpr int SPIN_CAP = 1 << 19;

constexpr int G_EPOCH = 0;
constexpr int G_Q = 512;
constexpr int G_ML = G_Q + 8 * 4 * 512;
constexpr int G_Z = G_ML + 8 * 4 * 32 * 2;
constexpr int G_ACC = G_Z + 16 * 512;
constexpr int G_END = G_ACC + 256 * 512;
constexpr int G_DIAG = G_END;

__device__ __forceinline__ float bf2f(u16 b) { return __uint_as_float((unsigned)b << 16); }
__device__ __forceinline__ float rbf(float x) { return (float)(__bf16)x; }
__device__ __forceinline__ float asf(unsigned w) { return __uint_as_float(w); }
__device__ __forceinline__ unsigned cvtpk(float lo, float hi) {
  bf16x2 r = {(__bf16)lo, (__bf16)hi};
  return __builtin_bit_cast(unsigned, r);
}
__device__ __forceinline__ f32x2 unpk(unsigned w) { return f32x2{asf(w << 16), asf(w & 0xffff0000u)}; }
__device__ __forceinline__ f32x2 rbf2(f32x2 v) {
  const unsigned p = cvtpk(v[0], v[1]);
  return f32x2{asf(__builtin_amdgcn_perm(p, p, 0x01000c0cu)), asf(__builtin_amdgcn_perm(p, p, 0x03020c0cu))};
}
__device__ __forceinline__ float dot2(unsigned a, unsigned b, float c) {
  return __builtin_amdgcn_fdot2_f32_bf16(__builtin_bit_cast(bf16x2, a), __builtin_bit_cast(bf16x2, b), c, false);
}
__device__ __forceinline__ f32x4 mfma(v4u a, v4u b, f32x4 c) {
  return __builtin_amdgcn_mfma_f32_16x16x32_bf16(__builtin_bit_cast(bf16x8, a), __builtin_bit_cast(bf16x8, b), c, 0, 0, 0);
}
template <int CTRL> __device__ __forceinline__ float dpp(float x) {
  return __builtin_bit_cast(float, __builtin_amdgcn_mov_dpp(__builtin_bit_cast(int, x), CTRL, 0xf, 0xf, true));
}
template <int CTRL, int RM> __device__ __forceinline__ float dppz(float x) {
  return __builtin_bit_cast(float, __builtin_amdgcn_update_dpp(0, __builtin_bit_cast(int, x), CTRL, RM, 0xf, false));
}
constexpr int XOR1 = 0xB1, XOR2 = 0x4E, ROR8 = 0x128, ROR4 = 0x124, ROR12 = 0x12C, HMIR = 0x141, BC15 = 0x142, BC31 = 0x143;
__device__ __forceinline__ float swap32_add(float a, float b) {
  auto s = __builtin_amdgcn_permlane32_swap(__float_as_uint(a), __float_as_uint(b), false, false);
  return asf(s[0]) + asf(s[1]);
}
__device__ __forceinline__ float swap16_add(float a, float b) {
  auto s = __builtin_amdgcn_permlane16_swap(__float_as_uint(a), __float_as_uint(b), false, false);
  return asf(s[0]) + asf(s[1]);
}
__device__ __forceinline__ float swap16_max(float x) {
  auto s = __builtin_amdgcn_permlane16_swap(__float_as_uint(x), __float_as_uint(x), false, false);
  return fmaxf(asf(s[0]), asf(s[1]));
}
__device__ __forceinline__ float xrow_sum(float x) { x = swap16_add(x, x); return swap32_add(x, x); }
__device__ __forceinline__ float xrow_max(float x) {
  x = swap16_max(x);
  auto t = __builtin_amdgcn_permlane32_swap(__float_as_uint(x), __float_as_uint(x), false, false);
  return fmaxf(asf(t[0]), asf(t[1]));
}
__device__ __forceinline__ float rdlane(float x, int l) { return __builtin_bit_cast(float, __builtin_amdgcn_readlane(__builtin_bit_cast(int, x), l)); }
__device__ __forceinline__ void wave_sum2_l63(float& a, float& b) {
  a += dpp<XOR1>(a); b += dpp<XOR1>(b);
  a += dpp<XOR2>(a); b += dpp<XOR2>(b);
  a += dpp<ROR4>(a); b += dpp<ROR4>(b);
  a += dpp<ROR8>(a); b += dpp<ROR8>(b);
  a += dppz<BC15, 0xa>(a); b += dppz<BC15, 0xa>(b);
  a += dppz<BC31, 0xc>(a); b += dppz<BC31, 0xc>(b);
}
__device__ __forceinline__ void ln_finish(float s1, float s2, float c, float& mb, float& rs) {
  wave_sum2_l63(s1, s2);
  const float dm = rdlane(s1, 63) * (1.f / D);
  const float var = fmaxf(rdlane(s2, 63) * (1.f / D) - dm * dm, 0.f);
  const f32x2 mv = rbf2(f32x2{c + dm, var});
  mb = mv[0];
  rs = __builtin_amdgcn_rcpf(rbf(__builtin_amdgcn_sqrtf(rbf(mv[1] + EPS))));
}
__device__ __forceinline__ void ln_stats(const f32x2 (&v)[4], float& mb, float& rs) {
  const float c = rdlane(v[0][0], 0);
  const f32x2 d0 = v[0] - c, d1 = v[1] - c, d2 = v[2] - c, d3 = v[3] - c;
  const f32x2 s1 = (d0 + d1) + (d2 + d3), s2 = (d0 * d0 + d1 * d1) + (d2 * d2 + d3 * d3);
  ln_finish(s1[0] + s1[1], s2[0] + s2[1], c, mb, rs);
}
__device__ __forceinline__ unsigned ln_apply_pk(f32x2 v, float mb, float rs) { const f32x2 t = rbf2(v - mb) * rs; return cvtpk(t[0], t[1]); }

constexpr int AUX_SC1 = 16, AUX_NT = 2;
__device__ __forceinline__ v4u get2(rsrc_t rs, int voff, int soff) { return __builtin_amdgcn_raw_buffer_load_b128(rs, voff, soff, AUX_SC1); }
__device__ __forceinline__ void put2(rsrc_t rs, int voff, int soff, float a, float b, unsigned tag) {
  __builtin_amdgcn_raw_buffer_store_b128(v4u{tag, __float_as_uint(a), tag, __float_as_uint(b)}, rs, voff + soff, 0, AUX_SC1);
}
__device__ __forceinline__ unsigned stale(v4u g, unsigned tag) { return (g[0] ^ tag) | (g[2] ^ tag); }
__device__ __forceinline__ bool any(unsigned bad) {
  asm("" : "+v"(bad));
  return __builtin_amdgcn_ballot_w64(bad != 0u) != 0ull;
}
template <int I, int N> struct Loop {
  template <class F> static __device__ __forceinline__ void run(F&& f) {
    if constexpr (I < N) { f(std::integral_constant<int, I>{}); Loop<I + 1, N>::run(f); }
  }
};
template <int N, class LD, class NAP> __device__ __forceinline__ void gather(v4u (&g)[N], unsigned tag, LD&& ld, NAP&& nap) {
  unsigned pend = (1u << N) - 1u;
#pragma unroll 1
  for (int spin = 0; spin < SPIN_CAP; ++spin) {
    asm volatile("" ::: "memory");
    Loop<0, N>::run([&](auto I) { if (pend & (1u << I.value)) g[I.value] = ld(I); });
    Loop<0, N>::run([&](auto I) { if (pend & (1u << I.value)) { if (!any(stale(g[I.value], tag))) pend &= ~(1u << I.value); } });
    if (pend == 0u) break;
    nap(pend);
  }
}
#define G16_LD(S, J) ".set gi_, " #J "\n\t.rept 4\n\tbuffer_load_dwordx4 v[128+4*gi_:131+4*gi_], %[vo], %[rs], " S " offen offset:(gi_&3)*1024 sc1\n\t.set gi_, gi_+1\n\t.endr\n\t"
#define G16_RE(S, J) ".set gi_, " #J "\n\t.rept 4\n\tv_cmpx_ne_u32_e32 vcc, 0, v[108+gi_]\n\tbuffer_load_dwordx4 v[128+4*gi_:131+4*gi_], %[vo], %[rs], " S " offen offset:(gi_&3)*1024 sc1\n\ts_mov_b64 exec, -1\n\t.set gi_, gi_+1\n\t.endr\n\t"
template <int SLP> __device__ __forceinline__ void gather16(v4u (&g)[16], rsrc_t rs, int voff, int soff, unsigned tag) {
  unsigned spin = SPIN_CAP;
  const int s0 = __builtin_amdgcn_readfirstlane(soff);
  asm volatile(
      "s_nop 4\n\t"
      G16_LD("%[s0]", 0) G16_LD("%[s1]", 4) G16_LD("%[s2]", 8) G16_LD("%[s3]", 12)
      "1:\n\t"
      "s_waitcnt vmcnt(0)\n\t"
      ".set gi_, 0\n\t.rept 16\n\tv_bitop3_b32 v[108+gi_], v[128+4*gi_], v[130+4*gi_], %[tg] bitop3:0x7e\n\t.set gi_, gi_+1\n\t.endr\n\t"
      "v_or3_b32 v124, v108, v109, v110\n\t"
      ".set gi_, 3\n\t.rept 6\n\tv_or3_b32 v124, v124, v[108+gi_], v[109+gi_]\n\t.set gi_, gi_+2\n\t.endr\n\t"
      "v_or_b32_e32 v124, v124, v123\n\t"
      "v_cmp_ne_u32_e32 vcc, 0, v124\n\t"
      "s_cbranch_vccz 2f\n\t"
      "s_sub_u32 %[sp], %[sp], 1\n\t"
      "s_cbranch_scc1 2f\n\t"
      "s_sleep %[sl]\n\t"
      G16_RE("%[s0]", 0) G16_RE("%[s1]", 4) G16_RE("%[s2]", 8) G16_RE("%[s3]", 12)
      "s_branch 1b\n\t"
      "2:"
      : "={v[128:131]}"(g[0]), "={v[132:135]}"(g[1]), "={v[136:139]}"(g[2]), "={v[140:143]}"(g[3]), "={v[144:147]}"(g[4]), "={v[148:151]}"(g[5]),
        "={v[152:155]}"(g[6]), "={v[156:159]}"(g[7]), "={v[160:163]}"(g[8]), "={v[164:167]}"(g[9]), "={v[168:171]}"(g[10]), "={v[172:175]}"(g[11]),
        "={v[176:179]}"(g[12]), "={v[180:183]}"(g[13]), "={v[184:187]}"(g[14]), "={v[188:191]}"(g[15]), [sp] "+s"(spin)
      : [vo] "{v125}"(voff), [rs] "s"(rs), [s0] "s"(s0), [s1] "s"(s0 + 4096), [s2] "s"(s0 + 8192), [s3] "s"(s0 + 12288), [tg] "s"(tag), [sl] "i"(SLP)
      : "v108", "v109", "v110", "v111", "v112", "v113", "v114", "v115", "v116", "v117", "v118", "v119", "v120", "v121", "v122", "v123", "v124",
        "vcc", "scc", "memory");
}
__device__ __forceinline__ u64 rt() { return __builtin_amdgcn_s_memrealtime(); }

constexpr int BLK_STRIDE = 4096;
template <int I> __device__ __forceinline__ v4u ldkv(rsrc_t rk, rsrc_t rv, int koff, int voff) {
  if constexpr (I < NLK) return __builtin_amdgcn_raw_buffer_load_b128(rk, koff + (I & 3) * 1024, (I >> 2) * BLK_STRIDE, KAUX);
  else return __builtin_amdgcn_raw_buffer_load_b128(rv, voff + ((I - NLK) & 3) * 1024, ((I - NLK) >> 2) * BLK_STRIDE, VAUX);
}
template <int BM> __device__ __forceinline__ float bsel(float old, float src) {
  return __builtin_bit_cast(float, __builtin_amdgcn_update_dpp(__builtin_bit_cast(int, old), __builtin_bit_cast(int, src), 0xE4, 0xf, BM, false));
}
#define STR2(x) #x
#define STR(x) STR2(x)
#define WSTAMP(k) do { } while (0)
#define STAMP(k) do { } while (0)
#define STAMPW(k, ww) do { } while (0)
#define NTL(p) __builtin_nontemporal_load(p)

constexpr int L_PART = 0;
constexpr int L_RED = L_PART + 8 * 512;
constexpr int L_WT = L_RED + 8 * 64;
constexpr int L_OB = L_WT + 4 * 32;
constexpr int L_MX = L_WT + 8 * 32;
constexpr int L_LS = L_MX + 32;
constexpr int L_SC = L_LS + 32;
constexpr int L_CNT = L_SC + 32;
constexpr int L_Q = L_CNT + 8;
constexpr int L_H = L_Q + 256;
constexpr int L_END = L_H + 512;

__global__ void __launch_bounds__(NTHR, 2) k_fused(const u16* __restrict__ x, const u16* __restrict__ kc, const u16* __restrict__ vc,
                                                   const u16* __restrict__ wq, u64* __restrict__ wsp, const u16* __restrict__ wo,
                                                   const u16* __restrict__ w1, const u16* __restrict__ b1, const u16* __restrict__ w2,
                                                   const u16* __restrict__ b2, u16* __restrict__ out) {
  __shared__ __attribute__((aligned(16))) float lds[L_END];
  gu64* ws = (gu64*)wsp;
  const int tid = threadIdx.x, lane = tid & 63, w = __builtin_amdgcn_readfirstlane(tid >> 6);
  const int par = blockIdx.x & 1, jx = blockIdx.x >> 3;
  const int g = (blockIdx.x & 6) + (jx >> 4), s = 16 * par + (jx & 15);
  STAMP(0);
  volatile LAS unsigned* cnt = (volatile LAS unsigned*)(lds + L_CNT);
  if (tid < 4) cnt[tid] = 0u;
  asm volatile("s_waitcnt lgkmcnt(0)\n\ts_barrier" :: "s"(x), "s"(kc), "s"(vc), "s"(wq), "s"(wsp) : "memory");
  unsigned tag;
  const rsrc_t rw = __builtin_amdgcn_make_buffer_rsrc((void*)wsp, 0, G_END * 8, 0x00020000);
  const int ntw = par ? TO : TE, tlo = (ntw * w) >> 3, nt = ((ntw * (w + 1)) >> 3) - tlo;
  const size_t key0 = (size_t)g * SK + 32 * ((par ? 16 * TE : 0) + (jx & 15) * ntw + tlo);
  const int lg = lane >> 4, bank = (lane >> 2) & 3, lh = lane & 3;
  const rsrc_t rk = __builtin_amdgcn_make_buffer_rsrc((void*)(kc + key0 * DH), 0, nt * 8192, 0x00020000);
  const rsrc_t rv = __builtin_amdgcn_make_buffer_rsrc((void*)(vc + key0 * DH), 0, nt * 8192, 0x00020000);
  const int koff = 16 * (16 * bank + 4 * lg + lh), voff = 16 * lane;
  v4u buf[NL];
  if (__builtin_expect(w == 0, 1)) {
    __builtin_amdgcn_s_setprio(3);
    const int kq = s >> 3, cq = 64 * (s & 7);
    const rsrc_t rq = __builtin_amdgcn_make_buffer_rsrc((void*)(wq + (size_t)kq * 128 * 4096 + 512 * g + cq), 0, 127 * 8192 + 128, 0x00020000);
    const v4u xv = NTL((const v4u*)(x + 8 * lane));
    const v4u xa = NTL((const v4u*)(x + 128 * kq + 16 * (lane >> 3))), xb = NTL((const v4u*)(x + 128 * kq + 16 * (lane >> 3) + 8));
    v4u wv[16];
    const int qoff = (lane >> 3) * (16 * 8192) + (lane & 7) * 16;
#pragma unroll
    for (int i = 0; i < 16; ++i) wv[i] = __builtin_amdgcn_raw_buffer_load_b128(rq, qoff, i * 8192, WAUX);
    const u64 epoch = *(const __attribute__((address_space(4))) u64*)(wsp + G_EPOCH);
    __builtin_amdgcn_sched_barrier(0);
    if (ORDB) asm volatile("s_barrier" ::: "memory");
    f32x2 v[4];
#pragma unroll
    for (int i = 0; i < 4; ++i) v[i] = unpk(xv[i]);
    float mb, rs;
    ln_stats(v, mb, rs);
    unsigned xn[8];
#pragma unroll
    for (int i = 0; i < 8; ++i) xn[i] = ln_apply_pk(unpk(i < 4 ? xa[i & 3] : xb[i & 3]), mb, rs);
    {
      u64 mixv = epoch + 0x9E3779B97F4A7C15ull;
      mixv = (mixv ^ (mixv >> 30)) * 0xBF58476D1CE4E5B9ull;
      mixv = (mixv ^ (mixv >> 27)) * 0x94D049BB133111EBull;
      mixv ^= mixv >> 31;
      const unsigned lo = (unsigned)mixv;
      tag = __builtin_amdgcn_readfirstlane(lo == (unsigned)epoch ? lo ^ 1u : lo);
      cnt[4] = tag;
      cnt[5] = (unsigned)(mixv >> 32);
    }
    float a[8] = {0.f, 0.f, 0.f, 0.f, 0.f, 0.f, 0.f, 0.f};
#pragma unroll
    for (int j = 0; j < 8; ++j)
#pragma unroll
      for (int d = 0; d < 4; ++d) {
        a[2 * d] = dot2(__builtin_amdgcn_perm(wv[2 * j + 1][d], wv[2 * j][d], 0x05040100u), xn[j], a[2 * d]);
        a[2 * d + 1] = dot2(__builtin_amdgcn_perm(wv[2 * j + 1][d], wv[2 * j][d], 0x07060302u), xn[j], a[2 * d + 1]);
      }
    float c0 = swap16_add(swap32_add(a[0], a[4]), swap32_add(a[2], a[6])), c1 = swap16_add(swap32_add(a[1], a[5]), swap32_add(a[3], a[7]));
    c0 += dpp<ROR8>(c0); c1 += dpp<ROR8>(c1);
    if ((lane & 8) == 0) put2(rw, (lane & 7) * 64 + (lane >> 4) * 16, (G_Q + (g * 4 + kq) * 512 + cq) * 8, c0, c1, tag);
    STAMP(1);
    v4u qv[16];
    gather16<SLPQ>(qv, rw, voff, (G_Q + g * 2048) * 8, tag);
#pragma unroll
    for (int i = 0; i < 4; ++i)
      ((unsigned*)(lds + L_Q))[64 * i + lane] = cvtpk((asf(qv[i][1]) + asf(qv[4 + i][1])) + (asf(qv[8 + i][1]) + asf(qv[12 + i][1])),
                                                      (asf(qv[i][3]) + asf(qv[4 + i][3])) + (asf(qv[8 + i][3]) + asf(qv[12 + i][3])));
    asm volatile("s_waitcnt lgkmcnt(0)" ::: "memory");
    cnt[1] = 1u;
    __builtin_amdgcn_s_setprio(0);
    STAMP(2);
    WSTAMP(0);
    Loop<0, W0>::run([&](auto I) { buf[I.value] = ldkv<I.value>(rk, rv, koff, voff); });
  } else {
    if (ORDB) asm volatile("s_barrier" ::: "memory");
    Loop<0, W0>::run([&](auto I) { buf[I.value] = ldkv<I.value>(rk, rv, koff, voff); });
    WSTAMP(0);
#pragma unroll 1
    for (int spin = 0; spin < (SPIN_CAP << 4) && __builtin_amdgcn_readfirstlane(cnt[1]) == 0u; ++spin) __builtin_amdgcn_s_sleep(2);
    asm volatile("" ::: "memory");
    tag = __builtin_amdgcn_readfirstlane(cnt[4]);
  }
  STAMPW(3, 1);
  const v4u qb = *(const v4u*)((const u16*)(lds + L_Q) + lh * DH + 8 * (4 * lg + bank));
  __builtin_amdgcn_sched_barrier(0);

  float X[NLK / 4];
  Loop<0, NLK / 4>::run([&](auto T) {
    constexpr int t = T.value;
    f32x4 d[4];
    Loop<0, 4>::run([&](auto M) {
      constexpr int i = 4 * t + M.value;
      d[M.value] = mfma(buf[i], qb, f32x4{0.f, 0.f, 0.f, 0.f});
      if constexpr (i + W0 < NLK + WV) buf[i + W0] = ldkv<i + W0>(rk, rv, koff, voff);
    });
    float R[4];
#pragma unroll
    for (int k = 0; k < 4; ++k) R[k] = bsel<8>(bsel<4>(bsel<2>(d[k][0], d[(k + 1) & 3][1]), d[(k + 2) & 3][2]), d[(k + 3) & 3][3]);
    X[t] = ((R[0] + dpp<ROR4>(R[1])) + dpp<ROR8>(R[2])) + dpp<ROR12>(R[3]);
    asm volatile("" : "+v"(X[t]));
    __builtin_amdgcn_sched_barrier(0);
  });
  STAMP(4);
  WSTAMP(1);
  unsigned PK[NLK / 8];
  {
    float m = -INFINITY;
#pragma unroll
    for (int t = 0; t < NLK / 8; ++t) {
      f32x2 sc = rbf2(rbf2(f32x2{X[2 * t], X[2 * t + 1]}) * SCALE);
      if (t >= TO / 8 && t >= nt) sc = f32x2{-INFINITY, -INFINITY};
      X[2 * t] = sc[0]; X[2 * t + 1] = sc[1];
      m = fmaxf(m, fmaxf(sc[0], sc[1]));
    }
    m = fmaxf(m, dpp<ROR4>(m)); m = fmaxf(m, dpp<ROR8>(m)); m = xrow_max(m);
    float l = 0.f;
#pragma unroll
    for (int t = 0; t < NLK / 4; ++t) { X[t] = __builtin_amdgcn_exp2f((X[t] - m) * LOG2E); l += X[t]; }
    l += dpp<ROR4>(l); l += dpp<ROR8>(l); l = xrow_sum(l);
#pragma unroll
    for (int t = 0; t < NLK / 8; ++t) PK[t] = cvtpk(X[2 * t], X[2 * t + 1]);
    unsigned old = 0u;
    if (lane < 4) { lds[L_MX + w * 4 + lane] = m; lds[L_LS + w * 4 + lane] = l; }
    if (lane == 0) old = __hip_atomic_fetch_add((unsigned*)(lds + L_CNT), 1u, __ATOMIC_ACQ_REL, __HIP_MEMORY_SCOPE_WORKGROUP);
    if (__builtin_amdgcn_readfirstlane(old) == NWAVE - 1) {
      const float mw = lds[L_MX + (lane & 31)], lw = lds[L_LS + (lane & 31)];
      float M = fmaxf(mw, dpp<ROR4>(mw)); M = fmaxf(M, dpp<ROR8>(M)); M = swap16_max(M);
      const float sc = __builtin_amdgcn_exp2f((mw - M) * LOG2E);
      float L = lw * sc;
      L += dpp<ROR4>(L); L += dpp<ROR8>(L); L = swap16_add(L, L);
      if (lane < 32) lds[L_SC + lh * 8 + (lane >> 2)] = sc;
      if (lane < 4) put2(rw, lane * 512, (G_ML + g * 256 + s * 2) * 8, M, L, tag);
    }
  }
  const int hp = s & 1, cbk = (s >> 1) & 7;
  const rsrc_t ro = __builtin_amdgcn_make_buffer_rsrc((void*)(wo + (size_t)(512 * g + 256 * hp + 64 * (w & 3)) * D + 64 * cbk), 0, par ? 0 : 63 * 1024 + 128, 0x00020000);
  v4u wov[8], w1v, b1v, w2v;
  const unsigned xt = NTL((const unsigned*)(x + 128 * (w & 3) + 2 * lane));
  const u16 b216 = NTL(b2);

  unsigned bm[4];
#pragma unroll
  for (int mm = 0; mm < 4; ++mm) bm[mm] = bank == mm ? 0xffffffffu : 0u;
  f32x4 acc[8];
#pragma unroll
  for (int j = 0; j < 8; ++j) acc[j] = f32x4{0.f, 0.f, 0.f, 0.f};
  __builtin_amdgcn_sched_barrier(0);
  Loop<0, NLK / 8>::run([&](auto TT) {
    constexpr int T = TT.value;
    constexpr int base = NLK + 8 * T;
    v4u pa;
#pragma unroll
    for (int mm = 0; mm < 4; ++mm) pa[mm] = PK[T] & bm[mm];
#pragma unroll
    for (int jj = 0; jj < 4; ++jj) {
      v4u b0, b1;
#pragma unroll
      for (int mm = 0; mm < 4; ++mm) {
        b0[mm] = __builtin_amdgcn_perm(buf[base + 4 + mm][jj], buf[base + mm][jj], 0x05040100u);
        b1[mm] = __builtin_amdgcn_perm(buf[base + 4 + mm][jj], buf[base + mm][jj], 0x07060302u);
      }
      acc[2 * jj] = mfma(pa, b0, acc[2 * jj]);
      acc[2 * jj + 1] = mfma(pa, b1, acc[2 * jj + 1]);
    }
    __builtin_amdgcn_sched_barrier(0);
    Loop<0, 8>::run([&](auto I) {
      constexpr int idx = base + I.value;
      if constexpr (idx + WV < NL) buf[idx + WV] = ldkv<idx + WV>(rk, rv, koff, voff);
    });
    if constexpr (base + 8 + WV == NL + 8) {
      if (w < 4) {
#pragma unroll
        for (int i = 0; i < 8; ++i) wov[i] = __builtin_amdgcn_raw_buffer_load_b128(ro, (lane >> 3) * 8192 + (lane & 7) * 16, i * 1024, WAUX);
        const int fi = tid < FF / 8 ? tid : 0;
        w1v = NTL((const v4u*)(w1 + 8 * fi)); b1v = NTL((const v4u*)(b1 + 8 * fi)); w2v = NTL((const v4u*)(w2 + 8 * fi));
      }
    }
    __builtin_amdgcn_sched_barrier(0);
  });
  STAMP(5);
  WSTAMP(2);
  STAMPW(11, 7);
  {
    float r2[8];
#pragma unroll
    for (int j = 0; j < 8; ++j) r2[j] = swap16_add(swap32_add(acc[j][0], acc[j][2]), swap32_add(acc[j][1], acc[j][3]));
    *(f32x4*)(lds + L_PART + w * 512 + lane * 8) = f32x4{r2[0], r2[1], r2[2], r2[3]};
    *(f32x4*)(lds + L_PART + w * 512 + lane * 8 + 4) = f32x4{r2[4], r2[5], r2[6], r2[7]};
  }
  if (lane == 0) __hip_atomic_fetch_add((unsigned*)(lds + L_CNT) + 2, 1u, __ATOMIC_RELAXED, __HIP_MEMORY_SCOPE_WORKGROUP);
  f32x2 w1f[4], b1f[4];
  v4u wb[8];
  f32x4 wt[4];
  bool have_wt = false;
  auto mld = [&](auto) { return get2(rw, (lane & 31) * 16, (G_ML + (g * 4 + 2 * hp + ((w >> 1) & 1)) * 64) * 8); };
  auto weights = [&](v4u mv) {
    const float mp = asf(mv[1]);
    float M = fmaxf(mp, dpp<XOR1>(mp)); M = fmaxf(M, dpp<XOR2>(M)); M = fmaxf(M, dpp<ROR4>(M)); M = fmaxf(M, dpp<ROR8>(M)); M = swap16_max(M);
    const float e = __builtin_amdgcn_exp2f((mp - M) * LOG2E);
    float L = e * asf(mv[3]);
    L += dpp<XOR1>(L); L += dpp<XOR2>(L); L += dpp<ROR4>(L); L += dpp<ROR8>(L); L = swap16_add(L, L);
    lds[L_WT + w * 32 + (lane & 31)] = e * __builtin_amdgcn_rcpf(L);
#pragma unroll
    for (int i = 0; i < 4; ++i) wt[i] = *(const f32x4*)(lds + L_WT + w * 32 + 16 * (lane >> 5) + 4 * i);
#pragma unroll
    for (int i = 0; i < 4; ++i) asm volatile("" : "+v"(wt[i]));
  };
  if (w < 4) {
#pragma unroll
    for (int i = 0; i < 4; ++i) { w1f[i] = unpk(w1v[i]); b1f[i] = unpk(b1v[i]); }
    if (tid >= FF / 8) w2v = v4u{0u, 0u, 0u, 0u};
#pragma unroll
    for (int i = 0; i < 4; ++i) asm volatile("" : "+v"(w1f[i]), "+v"(b1f[i]));
    asm volatile("" : "+v"(w2v));
    if (par == 0) {
#pragma unroll
      for (int jj = 0; jj < 4; ++jj)
#pragma unroll
        for (int mm = 0; mm < 4; ++mm) {
          wb[2 * jj][mm] = __builtin_amdgcn_perm(wov[2 * mm + 1][jj], wov[2 * mm][jj], 0x05040100u);
          wb[2 * jj + 1][mm] = __builtin_amdgcn_perm(wov[2 * mm + 1][jj], wov[2 * mm][jj], 0x07060302u);
        }
#pragma unroll
      for (int i = 0; i < 8; ++i) asm volatile("" : "+v"(wb[i]));
      unsigned done = 0u;
#pragma unroll 1
      for (int spin = 0; spin < (SPIN_CAP << 4) && (done = __builtin_amdgcn_readfirstlane(cnt[2])) < GATE1; ++spin) __builtin_amdgcn_s_sleep(8);
      if (done < NWAVE - 1) {
        const v4u mv = mld(0);
        if (!any(stale(mv, tag))) { weights(mv); have_wt = true; }
      }
    }
  }
  WSTAMP(3);
  __syncthreads();
  if (w < 4) {
    const f32x4 sa = *(const f32x4*)(lds + L_SC + w * 8), sb = *(const f32x4*)(lds + L_SC + w * 8 + 4);
    f32x2 o0 = *(const f32x2*)(lds + L_PART + 2 * tid) * sa[0], o1 = *(const f32x2*)(lds + L_PART + 4 * 512 + 2 * tid) * sb[0];
#pragma unroll
    for (int i = 1; i < 4; ++i) {
      o0 += *(const f32x2*)(lds + L_PART + i * 512 + 2 * tid) * sa[i];
      o1 += *(const f32x2*)(lds + L_PART + (4 + i) * 512 + 2 * tid) * sb[i];
    }
    o0 += o1;
    put2(rw, (tid >> 5) * 16384 + (tid & 31) * 16, (G_ACC + g * 32 * 512 + (2 * (s & 15) + par) * 64) * 8, o0[0], o0[1], tag);
  }
  STAMP(6);
  if (par == 0) {
    __builtin_amdgcn_s_barrier();
    if (w < 4) {
      if (!have_wt) {
        v4u mv[1];
        gather<1>(mv, tag, mld, [](unsigned) { __builtin_amdgcn_s_sleep(8); });
        weights(mv[0]);
      }
      v4u av[16];
      gather16<SLP1>(av, rw, voff, (G_ACC + (g * 8 + hp * 4 + w) * 2048) * 8, tag);
      float oa[2] = {0.f, 0.f}, ob[2] = {0.f, 0.f};
#pragma unroll
      for (int i = 0; i < 16; ++i) { oa[i & 1] = fmaf(asf(av[i][1]), wt[i >> 2][i & 3], oa[i & 1]); ob[i & 1] = fmaf(asf(av[i][3]), wt[i >> 2][i & 3], ob[i & 1]); }
      const float o = swap32_add(oa[0] + oa[1], ob[0] + ob[1]);
      u16* obp = (u16*)(lds + L_OB) + 64 * w;
      obp[2 * (lane & 31) + (lane >> 5)] = (u16)cvtpk(o, o);
      STAMP(7);
      const v4u af = *(const v4u*)(obp + 16 * lg + 8 * (lane & 1));
      float z[8];
#pragma unroll
      for (int j = 0; j < 8; ++j) { const f32x4 d = mfma(af, wb[j], f32x4{0.f, 0.f, 0.f, 0.f}); z[j] = bsel<0xC>(d[0], d[1]); }
      if (lane < 16) {
        *(f32x4*)(lds + L_RED + w * 128 + lane * 8) = f32x4{z[0], z[1], z[2], z[3]};
        *(f32x4*)(lds + L_RED + w * 128 + lane * 8 + 4) = f32x4{z[4], z[5], z[6], z[7]};
      }
    }
    __syncthreads();
    if (tid < 32) {
      f32x2 t[8];
#pragma unroll
      for (int i = 0; i < 8; ++i) t[i] = *(const f32x2*)(lds + L_RED + i * 64 + 2 * lane);
      const f32x2 zz = ((t[0] + t[1]) + (t[2] + t[3])) + ((t[4] + t[5]) + (t[6] + t[7]));
      put2(rw, voff, (G_Z + ((cbk >> 1) * 16 + 2 * g + hp) * 128 + (cbk & 1) * 64) * 8, zz[0], zz[1], tag);
    }
  }
  __builtin_amdgcn_s_barrier();
  STAMP(8);
  if (w >= 4) {
    v4u zv[16];
    if (SLPZ && par) __builtin_amdgcn_s_sleep(SLPZ);
    gather16<SLP2>(zv, rw, voff, (G_Z + (w & 3) * 2048) * 8, tag);
    float ta[8], tb[8];
#pragma unroll
    for (int i = 0; i < 8; ++i) { ta[i] = asf(zv[2 * i][1]) + asf(zv[2 * i + 1][1]); tb[i] = asf(zv[2 * i][3]) + asf(zv[2 * i + 1][3]); }
    const f32x2 t = {((ta[0] + ta[1]) + (ta[2] + ta[3])) + ((ta[4] + ta[5]) + (ta[6] + ta[7])), ((tb[0] + tb[1]) + (tb[2] + tb[3])) + ((tb[4] + tb[5]) + (tb[6] + tb[7]))};
    *(f32x2*)(lds + L_H + 128 * (w & 3) + 2 * lane) = rbf2(unpk(xt) + rbf2(t));
  }
  __syncthreads();
  STAMP(9);
  if (w >= 4) return;
  {
    const f32x4 ha = *(const f32x4*)(lds + L_H + 8 * lane), hb = *(const f32x4*)(lds + L_H + 8 * lane + 4);
    const int c0 = 2 * blockIdx.x;
    const f32x2 hc = *(const f32x2*)(lds + L_H + c0);
    const f32x2 v[4] = {f32x2{ha[0], ha[1]}, f32x2{ha[2], ha[3]}, f32x2{hb[0], hb[1]}, f32x2{hb[2], hb[3]}};
    float mb1, rs1, mb2, rs2;
    ln_stats(v, mb1, rs1);
    unsigned y1[4];
#pragma unroll
    for (int i = 0; i < 4; ++i) y1[i] = ln_apply_pk(v[i], mb1, rs1);
    const f32x2 n1 = rbf2(rbf2(hc - mb1) * rs1);
    ln_finish(dot2(y1[0], 0x3f803f80u, dot2(y1[1], 0x3f803f80u, 0.f)) + dot2(y1[2], 0x3f803f80u, dot2(y1[3], 0x3f803f80u, 0.f)),
              dot2(y1[0], y1[0], dot2(y1[1], y1[1], 0.f)) + dot2(y1[2], y1[2], dot2(y1[3], y1[3], 0.f)), 0.f, mb2, rs2);
    const f32x2 n = rbf2(rbf2(n1 - mb2) * rs2);
    constexpr float K1 = -2.f * LOG2E * 0.7978845608028654f, K2 = K1 * 0.044715f;
    float y[2] = {0.f, 0.f};
#pragma unroll
    for (int i = 0; i < 4; ++i)
#pragma unroll
      for (int c = 0; c < 2; ++c) {
        const f32x2 u = rbf2(rbf2(w1f[i] * n[c]) + b1f[i]);
        const f32x2 a = u * (u * u * K2 + K1);
        const f32x2 e = f32x2{__builtin_amdgcn_exp2f(a[0]), __builtin_amdgcn_exp2f(a[1])} + 1.f;
        const f32x2 gl = u * f32x2{__builtin_amdgcn_rcpf(e[0]), __builtin_amdgcn_rcpf(e[1])};
        y[c] = dot2(cvtpk(gl[0], gl[1]), w2v[i], y[c]);
      }
    wave_sum2_l63(y[0], y[1]);
    if (lane == 63) *(f32x2*)(lds + L_RED + 2 * w) = f32x2{y[0], y[1]};
    __syncthreads();
    if (tid == 0) {
      const f32x4 ta = *(const f32x4*)(lds + L_RED), tb = *(const f32x4*)(lds + L_RED + 4);
      const f32x2 ysum = (f32x2{ta[0], ta[1]} + f32x2{ta[2], ta[3]}) + (f32x2{tb[0], tb[1]} + f32x2{tb[2], tb[3]});
      const f32x2 r = hc + rbf2(rbf2(ysum) + bf2f(b216));
      *(unsigned*)(out + c0) = cvtpk(r[0], r[1]);
      if (blockIdx.x == 0) __hip_atomic_store(ws + G_EPOCH, ((u64)cnt[5] << 32) | tag, RLX_AGENT);
    }
  }
  STAMP(10);
}

extern "C" void kernel_launch(void* const* d_in, const int* in_sizes, int n_in, void* d_out, int out_size, void* d_ws, size_t ws_size,
                              hipStream_t stream) {
  k_fused<<<NWG, NTHR, 0, stream>>>((const u16*)d_in[0], (const u16*)d_in[1], (const u16*)d_in[2], (const u16*)d_in[3], (u64*)d_ws, (const u16*)d_in[4],
                                    (const u16*)d_in[6], (const u16*)d_in[7], (const u16*)d_in[8], (const u16*)d_in[9], (u16*)d_out);
}
```

```cpp
#define PROBE 0
#include <hip/hip_runtime.h>
#include <hip/hip_bf16.h>
#include <cstdint>
#include <type_traits>

#define KAUX 2
#define VAUX 2
#define WAUX 2
#define W0 32
#define WV 32
#define THR 40
#define TILES_EVEN 68
#define SLPL 32
#define SLPQ 0
#define SLPZ 0
#define ORDB 0
#define GATE1 5
#define MANY1 8
#define MANY2 12
#define SLP1 1
#define SLP2 1
#define CODEPF 0
#define PAT 0
#define UNTHR 1
#define DIAG 0

typedef unsigned v4u __attribute__((ext_vector_type(4)));
typedef unsigned v2u __attribute__((ext_vector_type(2)));
typedef float f32x4 __attribute__((ext_vector_type(4)));
typedef float f32x2 __attribute__((ext_vector_type(2)));
typedef __bf16 bf16x8 __attribute__((ext_vector_type(8)));
typedef __bf16 bf16x2 __attribute__((ext_vector_type(2)));
typedef unsigned short u16;
typedef unsigned long long u64;
typedef __amdgpu_buffer_rsrc_t rsrc_t;
#define GAS __attribute__((address_space(1)))
#define LAS __attribute__((address_space(3)))
typedef GAS u64 gu64;
#define RLX_AGENT __ATOMIC_RELAXED, __HIP_MEMORY_SCOPE_AGENT

constexpr int D = 512, SK = 65536, DH = 128, FF = 1792;
constexpr int NSPLIT = 32, NWAVE = 8, NTHR = 512, NWG = 256;
constexpr int TE = TILES_EVEN, TO = 128 - TE;
constexpr int NTILE = (TE + 7) / 8;
constexpr int NLK = NTILE * 8;
constexpr int NL = 2 * NLK;
constexpr float EPS = 1e-5f;
constexpr float SCALE = 0.08838834764831845f;
constexpr float LOG2E = 1.4426950408889634f;
constexpr int SPIN_CAP = 1 << 19;

constexpr int G_EPOCH = 0;
constexpr int G_Q = 512;
constexpr int G_ML = G_Q + 8 * 4 * 512;
constexpr int G_Z = G_ML + 8 * 4 * 32 * 2;
constexpr int G_ACC = G_Z + 16 * 512;
constexpr int G_END = G_ACC + 256 * 512;
constexpr int G_DIAG = G_END;

__device__ __forceinline__ float bf2f(u16 b) { return __uint_as_float((unsigned)b << 16); }
__device__ __forceinline__ float rbf(float x) { return (float)(__bf16)x; }
__device__ __forceinline__ float asf(unsigned w) { return __uint_as_float(w); }
__device__ __forceinline__ unsigned cvtpk(float lo, float hi) {
  bf16x2 r = {(__bf16)lo, (__bf16)hi};
  return __builtin_bit_cast(unsigned, r);
}
__device__ __forceinline__ f32x2 unpk(unsigned w) { return f32x2{asf(w << 16), asf(w & 0xffff0000u)}; }
__device__ __forceinline__ f32x2 rbf2(f32x2 v) {
  const unsigned p = cvtpk(v[0], v[1]);
  return f32x2{asf(__builtin_amdgcn_perm(p, p, 0x01000c0cu)), asf(__builtin_amdgcn_perm(p, p, 0x03020c0cu))};
}
__device__ __forceinline__ float dot2(unsigned a, unsigned b, float c) {
  return __builtin_amdgcn_fdot2_f32_bf16(__builtin_bit_cast(bf16x2, a), __builtin_bit_cast(bf16x2, b), c, false);
}
__device__ __forceinline__ f32x4 mfma(v4u a, v4u b, f32x4 c) {
  return __builtin_amdgcn_mfma_f32_16x16x32_bf16(__builtin_bit_cast(bf16x8, a), __builtin_bit_cast(bf16x8, b), c, 0, 0, 0);
}
template <int CTRL> __device__ __forceinline__ float dpp(float x) {
  return __builtin_bit_cast(float, __builtin_amdgcn_mov_dpp(__builtin_bit_cast(int, x), CTRL, 0xf, 0xf, true));
}
template <int CTRL, int RM> __device__ __forceinline__ float dppz(float x) {
  return __builtin_bit_cast(float, __builtin_amdgcn_update_dpp(0, __builtin_bit_cast(int, x), CTRL, RM, 0xf, false));
}
constexpr int XOR1 = 0xB1, XOR2 = 0x4E, ROR8 = 0x128, ROR4 = 0x124, ROR12 = 0x12C, HMIR = 0x141, BC15 = 0x142, BC31 = 0x143;
__device__ __forceinline__ float swap32_add(float a, float b) {
  auto s = __builtin_amdgcn_permlane32_swap(__float_as_uint(a), __float_as_uint(b), false, false);
  return asf(s[0]) + asf(s[1]);
}
__device__ __forceinline__ float swap16_add(float a, float b) {
  auto s = __builtin_amdgcn_permlane16_swap(__float_as_uint(a), __float_as_uint(b), false, false);
  return asf(s[0]) + asf(s[1]);
}
__device__ __forceinline__ float swap16_max(float x) {
  auto s = __builtin_amdgcn_permlane16_swap(__float_as_uint(x), __float_as_uint(x), false, false);
  return fmaxf(asf(s[0]), asf(s[1]));
}
__device__ __forceinline__ float xrow_sum(float x) { x = swap16_add(x, x); return swap32_add(x, x); }
__device__ __forceinline__ float xrow_max(float x) {
  x = swap16_max(x);
  auto t = __builtin_amdgcn_permlane32_swap(__float_as_uint(x), __float_as_uint(x), false, false);
  return fmaxf(asf(t[0]), asf(t[1]));
}
__device__ __forceinline__ float rdlane(float x, int l) { return __builtin_bit_cast(float, __builtin_amdgcn_readlane(__builtin_bit_cast(int, x), l)); }
__device__ __forceinline__ void wave_sum2_l63(float& a, float& b) {
  a += dpp<XOR1>(a); b += dpp<XOR1>(b);
  a += dpp<XOR2>(a); b += dpp<XOR2>(b);
  a += dpp<ROR4>(a); b += dpp<ROR4>(b);
  a += dpp<ROR8>(a); b += dpp<ROR8>(b);
  a += dppz<BC15, 0xa>(a); b += dppz<BC15, 0xa>(b);
  a += dppz<BC31, 0xc>(a); b += dppz<BC31, 0xc>(b);
}
__device__ __forceinline__ void ln_finish(float s1, float s2, float c, float& mb, float& rs) {
  wave_sum2_l63(s1, s2);
  const float dm = rdlane(s1, 63) * (1.f / D);
  const float var = fmaxf(rdlane(s2, 63) * (1.f / D) - dm * dm, 0.f);
  const f32x2 mv = rbf2(f32x2{c + dm, var});
  mb = mv[0];
  rs = __builtin_amdgcn_rcpf(rbf(__builtin_amdgcn_sqrtf(rbf(mv[1] + EPS))));
}
__device__ __forceinline__ void ln_stats(const f32x2 (&v)[4], float& mb, float& rs) {
  const float c = rdlane(v[0][0], 0);
  const f32x2 d0 = v[0] - c, d1 = v[1] - c, d2 = v[2] - c, d3 = v[3] - c;
  const f32x2 s1 = (d0 + d1) + (d2 + d3), s2 = (d0 * d0 + d1 * d1) + (d2 * d2 + d3 * d3);
  ln_finish(s1[0] + s1[1], s2[0] + s2[1], c, mb, rs);
}
__device__ __forceinline__ unsigned ln_apply_pk(f32x2 v, float mb, float rs) { const f32x2 t = rbf2(v - mb) * rs; return cvtpk(t[0], t[1]); }

constexpr int AUX_SC1 = 16, AUX_NT = 2;
__device__ __forceinline__ v4u get2(rsrc_t rs, int voff, int soff) { return __builtin_amdgcn_raw_buffer_load_b128(rs, voff, soff, AUX_SC1); }
__device__ __forceinline__ void put2(rsrc_t rs, int voff, int soff, float a, float b, unsigned tag) {
  __builtin_amdgcn_raw_buffer_store_b128(v4u{tag, __float_as_uint(a), tag, __float_as_uint(b)}, rs, voff + soff, 0, AUX_SC1);
}
__device__ __forceinline__ unsigned stale(v4u g, unsigned tag) { return (g[0] ^ tag) | (g[2] ^ tag); }
__device__ __forceinline__ bool any(unsigned bad) {
  asm("" : "+v"(bad));
  return __builtin_amdgcn_ballot_w64(bad != 0u) != 0ull;
}
template <int I, int N> struct Loop {
  template <class F> static __device__ __forceinline__ void run(F&& f) {
    if constexpr (I < N) { f(std::integral_constant<int, I>{}); Loop<I + 1, N>::run(f); }
  }
};
template <int N, class LD, class NAP> __device__ __forceinline__ void gather(v4u (&g)[N], unsigned tag, LD&& ld, NAP&& nap) {
  unsigned pend = (1u << N) - 1u;
#pragma unroll 1
  for (int spin = 0; spin < SPIN_CAP; ++spin) {
    asm volatile("" ::: "memory");
    Loop<0, N>::run([&](auto I) { if (pend & (1u << I.value)) g[I.value] = ld(I); });
    Loop<0, N>::run([&](auto I) { if (pend & (1u << I.value)) { if (!any(stale(g[I.value], tag))) pend &= ~(1u << I.value); } });
    if (pend == 0u) break;
    nap(pend);
  }
}
#define G16_LD(S, J) ".set gi_, " #J "\n\t.rept 4\n\tbuffer_load_dwordx4 v[128+4*gi_:131+4*gi_], %[vo], %[rs], " S " offen offset:(gi_&3)*1024 sc1\n\t.set gi_, gi_+1\n\t.endr\n\t"
#define G16_RE(S, J) ".set gi_, " #J "\n\t.rept 4\n\tv_cmpx_ne_u32_e32 vcc, 0, v[108+gi_]\n\tbuffer_load_dwordx4 v[128+4*gi_:131+4*gi_], %[vo], %[rs], " S " offen offset:(gi_&3)*1024 sc1\n\ts_mov_b64 exec, -1\n\t.set gi_, gi_+1\n\t.endr\n\t"
template <int SLP> __device__ __forceinline__ void gather16(v4u (&g)[16], rsrc_t rs, int voff, int soff, unsigned tag) {
  unsigned spin = SPIN_CAP;
  const int s0 = __builtin_amdgcn_readfirstlane(soff);
  asm volatile(
      "s_nop 4\n\t"
      G16_LD("%[s0]", 0) G16_LD("%[s1]", 4) G16_LD("%[s2]", 8) G16_LD("%[s3]", 12)
      "1:\n\t"
      "s_waitcnt vmcnt(0)\n\t"
      ".set gi_, 0\n\t.rept 16\n\tv_bitop3_b32 v[108+gi_], v[128+4*gi_], v[130+4*gi_], %[tg] bitop3:0x7e\n\t.set gi_, gi_+1\n\t.endr\n\t"
      "v_or3_b32 v124, v108, v109, v110\n\t"
      ".set gi_, 3\n\t.rept 6\n\tv_or3_b32 v124, v124, v[108+gi_], v[109+gi_]\n\t.set gi_, gi_+2\n\t.endr\n\t"
      "v_or_b32_e32 v124, v124, v123\n\t"
      "v_cmp_ne_u32_e32 vcc, 0, v124\n\t"
      "s_cbranch_vccz 2f\n\t"
      "s_sub_u32 %[sp], %[sp], 1\n\t"
      "s_cbranch_scc1 2f\n\t"
      "s_sleep %[sl]\n\t"
      G16_RE("%[s0]", 0) G16_RE("%[s1]", 4) G16_RE("%[s2]", 8) G16_RE("%[s3]", 12)
      "s_branch 1b\n\t"
      "2:"
      : "={v[128:131]}"(g[0]), "={v[132:135]}"(g[1]), "={v[136:139]}"(g[2]), "={v[140:143]}"(g[3]), "={v[144:147]}"(g[4]), "={v[148:151]}"(g[5]),
        "={v[152:155]}"(g[6]), "={v[156:159]}"(g[7]), "={v[160:163]}"(g[8]), "={v[164:167]}"(g[9]), "={v[168:171]}"(g[10]), "={v[172:175]}"(g[11]),
        "={v[176:179]}"(g[12]), "={v[180:183]}"(g[13]), "={v[184:187]}"(g[14]), "={v[188:191]}"(g[15]), [sp] "+s"(spin)
      : [vo] "{v125}"(voff), [rs] "s"(rs), [s0] "s"(s0), [s1] "s"(s0 + 4096), [s2] "s"(s0 + 8192), [s3] "s"(s0 + 12288), [tg] "s"(tag), [sl] "i"(SLP)
      : "v108", "v109", "v110", "v111", "v112", "v113", "v114", "v115", "v116", "v117", "v118", "v119", "v120", "v121", "v122", "v123", "v124",
        "vcc", "scc", "memory");
}
__device__ __forceinline__ u64 rt() { return __builtin_amdgcn_s_memrealtime(); }

constexpr int BLK_STRIDE = 4096;
template <int I> __device__ __forceinline__ v4u ldkv(rsrc_t rk, rsrc_t rv, int koff, int voff) {
  if constexpr (I < NLK) return __builtin_amdgcn_raw_buffer_load_b128(rk, koff + (I & 3) * 1024, (I >> 2) * BLK_STRIDE, KAUX);
  else return __builtin_amdgcn_raw_buffer_load_b128(rv, voff + ((I - NLK) & 3) * 1024, ((I - NLK) >> 2) * BLK_STRIDE, VAUX);
}
template <int BM> __device__ __forceinline__ float bsel(float old, float src) {
  return __builtin_bit_cast(float, __builtin_amdgcn_update_dpp(__builtin_bit_cast(int, old), __builtin_bit_cast(int, src), 0xE4, 0xf, BM, false));
}
#define STR2(x) #x
#define STR(x) STR2(x)
#define WSTAMP(k) do { } while (0)
#define STAMPW(k, ww) do { } while (0)
#define STAMP(k) do { if ((k) == PROBE) tprobe = rt(); if ((k) == 10) { while (rt() - tprobe < 10000ull) __builtin_amdgcn_s_sleep(8); } } while (0)
#define NTL(p) __builtin_nontemporal_load(p)

constexpr int L_PART = 0;
constexpr int L_RED = L_PART + 8 * 512;
constexpr int L_WT = L_RED + 8 * 64;
constexpr int L_OB = L_WT + 4 * 32;
constexpr int L_MX = L_WT + 8 * 32;
constexpr int L_LS = L_MX + 32;
constexpr int L_SC = L_LS + 32;
constexpr int L_CNT = L_SC + 32;
constexpr int L_Q = L_CNT + 8;
constexpr int L_H = L_Q + 256;
constexpr int L_END = L_H + 512;

__global__ void __launch_bounds__(NTHR, 2) k_fused(const u16* __restrict__ x, const u16* __restrict__ kc, const u16* __restrict__ vc,
                                                   const u16* __restrict__ wq, u64* __restrict__ wsp, const u16* __restrict__ wo,
                                                   const u16* __restrict__ w1, const u16* __restrict__ b1, const u16* __restrict__ w2,
                                                   const u16* __restrict__ b2, u16* __restrict__ out) {
  __shared__ __attribute__((aligned(16))) float lds[L_END];
  gu64* ws = (gu64*)wsp;
  const int tid = threadIdx.x, lane = tid & 63, w = __builtin_amdgcn_readfirstlane(tid >> 6);
  const int par = blockIdx.x & 1, jx = blockIdx.x >> 3;
  const int g = (blockIdx.x & 6) + (jx >> 4), s = 16 * par + (jx & 15);
  u64 tprobe = 0;
  STAMP(0);
  volatile LAS unsigned* cnt = (volatile LAS unsigned*)(lds + L_CNT);
  if (tid < 4) cnt[tid] = 0u;
  asm volatile("s_waitcnt lgkmcnt(0)\n\ts_barrier" :: "s"(x), "s"(kc), "s"(vc), "s"(wq), "s"(wsp) : "memory");
  unsigned tag;
  const rsrc_t rw = __builtin_amdgcn_make_buffer_rsrc((void*)wsp, 0, G_END * 8, 0x00020000);
  const int ntw = par ? TO : TE, tlo = (ntw * w) >> 3, nt = ((ntw * (w + 1)) >> 3) - tlo;
  const size_t key0 = (size_t)g * SK + 32 * ((par ? 16 * TE : 0) + (jx & 15) * ntw + tlo);
  const int lg = lane >> 4, bank = (lane >> 2) & 3, lh = lane & 3;
  const rsrc_t rk = __builtin_amdgcn_make_buffer_rsrc((void*)(kc + key0 * DH), 0, nt * 8192, 0x00020000);
  const rsrc_t rv = __builtin_amdgcn_make_buffer_rsrc((void*)(vc + key0 * DH), 0, nt * 8192, 0x00020000);
  const int koff = 16 * (16 * bank + 4 * lg + lh), voff = 16 * lane;
  v4u buf[NL];
  if (__builtin_expect(w == 0, 1)) {
    __builtin_amdgcn_s_setprio(3);
    const int kq = s >> 3, cq = 64 * (s & 7);
    const rsrc_t rq = __builtin_amdgcn_make_buffer_rsrc((void*)(wq + (size_t)kq * 128 * 4096 + 512 * g + cq), 0, 127 * 8192 + 128, 0x00020000);
    const v4u xv = NTL((const v4u*)(x + 8 * lane));
    const v4u xa = NTL((const v4u*)(x + 128 * kq + 16 * (lane >> 3))), xb = NTL((const v4u*)(x + 128 * kq + 16 * (lane >> 3) + 8));
    v4u wv[16];
    const int qoff = (lane >> 3) * (16 * 8192) + (lane & 7) * 16;
#pragma unroll
    for (int i = 0; i < 16; ++i) wv[i] = __builtin_amdgcn_raw_buffer_load_b128(rq, qoff, i * 8192, WAUX);
    const u64 epoch = *(const __attribute__((address_space(4))) u64*)(wsp + G_EPOCH);
    __builtin_amdgcn_sched_barrier(0);
    if (ORDB) asm volatile("s_barrier" ::: "memory");
    f32x2 v[4];
#pragma unroll
    for (int i = 0; i < 4; ++i) v[i] = unpk(xv[i]);
    float mb, rs;
    ln_stats(v, mb, rs);
    unsigned xn[8];
#pragma unroll
    for (int i = 0; i < 8; ++i) xn[i] = ln_apply_pk(unpk(i < 4 ? xa[i & 3] : xb[i & 3]), mb, rs);
    {
      u64 mixv = epoch + 0x9E3779B97F4A7C15ull;
      mixv = (mixv ^ (mixv >> 30)) * 0xBF58476D1CE4E5B9ull;
      mixv = (mixv ^ (mixv >> 27)) * 0x94D049BB133111EBull;
      mixv ^= mixv >> 31;
      const unsigned lo = (unsigned)mixv;
      tag = __builtin_amdgcn_readfirstlane(lo == (unsigned)epoch ? lo ^ 1u : lo);
      cnt[4] = tag;
      cnt[5] = (unsigned)(mixv >> 32);
    }
    float a[8] = {0.f, 0.f, 0.f, 0.f, 0.f, 0.f, 0.f, 0.f};
#pragma unroll
    for (int j = 0; j < 8; ++j)
#pragma unroll
      for (int d = 0; d < 4; ++d) {
        a[2 * d] = dot2(__builtin_amdgcn_perm(wv[2 * j + 1][d], wv[2 * j][d], 0x05040100u), xn[j], a[2 * d]);
        a[2 * d + 1] = dot2(__builtin_amdgcn_perm(wv[2 * j + 1][d], wv[2 * j][d], 0x07060302u), xn[j], a[2 * d + 1]);
      }
    float c0 = swap16_add(swap32_add(a[0], a[4]), swap32_add(a[2], a[6])), c1 = swap16_add(swap32_add(a[1], a[5]), swap32_add(a[3], a[7]));
    c0 += dpp<ROR8>(c0); c1 += dpp<ROR8>(c1);
    if ((lane & 8) == 0) put2(rw, (lane & 7) * 64 + (lane >> 4) * 16, (G_Q + (g * 4 + kq) * 512 + cq) * 8, c0, c1, tag);
    STAMP(1);
    v4u qv[16];
    gather16<SLPQ>(qv, rw, voff, (G_Q + g * 2048) * 8, tag);
#pragma unroll
    for (int i = 0; i < 4; ++i)
      ((unsigned*)(lds + L_Q))[64 * i + lane] = cvtpk((asf(qv[i][1]) + asf(qv[4 + i][1])) + (asf(qv[8 + i][1]) + asf(qv[12 + i][1])),
                                                      (asf(qv[i][3]) + asf(qv[4 + i][3])) + (asf(qv[8 + i][3]) + asf(qv[12 + i][3])));
    asm volatile("s_waitcnt lgkmcnt(0)" ::: "memory");
    cnt[1] = 1u;
    __builtin_amdgcn_s_setprio(0);
    STAMP(2);
    WSTAMP(0);
    Loop<0, W0>::run([&](auto I) { buf[I.value] = ldkv<I.value>(rk, rv, koff, voff); });
  } else {
    if (ORDB) asm volatile("s_barrier" ::: "memory");
    Loop<0, W0>::run([&](auto I) { buf[I.value] = ldkv<I.value>(rk, rv, koff, voff); });
    WSTAMP(0);
#pragma unroll 1
    for (int spin = 0; spin < (SPIN_CAP << 4) && __builtin_amdgcn_readfirstlane(cnt[1]) == 0u; ++spin) __builtin_amdgcn_s_sleep(2);
    asm volatile("" ::: "memory");
    tag = __builtin_amdgcn_readfirstlane(cnt[4]);
  }
  STAMPW(3, 1);
  const v4u qb = *(const v4u*)((const u16*)(lds + L_Q) + lh * DH + 8 * (4 * lg + bank));
  __builtin_amdgcn_sched_barrier(0);

  float X[NLK / 4];
  Loop<0, NLK / 4>::run([&](auto T) {
    constexpr int t = T.value;
    f32x4 d[4];
    Loop<0, 4>::run([&](auto M) {
      constexpr int i = 4 * t + M.value;
      d[M.value] = mfma(buf[i], qb, f32x4{0.f, 0.f, 0.f, 0.f});
      if constexpr (i + W0 < NLK + WV) buf[i + W0] = ldkv<i + W0>(rk, rv, koff, voff);
    });
    float R[4];
#pragma unroll
    for (int k = 0; k < 4; ++k) R[k] = bsel<8>(bsel<4>(bsel<2>(d[k][0], d[(k + 1) & 3][1]), d[(k + 2) & 3][2]), d[(k + 3) & 3][3]);
    X[t] = ((R[0] + dpp<ROR4>(R[1])) + dpp<ROR8>(R[2])) + dpp<ROR12>(R[3]);
    asm volatile("" : "+v"(X[t]));
    __builtin_amdgcn_sched_barrier(0);
  });
  STAMP(4);
  WSTAMP(1);
  unsigned PK[NLK / 8];
  {
    float m = -INFINITY;
#pragma unroll
    for (int t = 0; t < NLK / 8; ++t) {
      f32x2 sc = rbf2(rbf2(f32x2{X[2 * t], X[2 * t + 1]}) * SCALE);
      if (t >= TO / 8 && t >= nt) sc = f32x2{-INFINITY, -INFINITY};
      X[2 * t] = sc[0]; X[2 * t + 1] = sc[1];
      m = fmaxf(m, fmaxf(sc[0], sc[1]));
    }
    m = fmaxf(m, dpp<ROR4>(m)); m = fmaxf(m, dpp<ROR8>(m)); m = xrow_max(m);
    float l = 0.f;
#pragma unroll
    for (int t = 0; t < NLK / 4; ++t) { X[t] = __builtin_amdgcn_exp2f((X[t] - m) * LOG2E); l += X[t]; }
    l += dpp<ROR4>(l); l += dpp<ROR8>(l); l = xrow_sum(l);
#pragma unroll
    for (int t = 0; t < NLK / 8; ++t) PK[t] = cvtpk(X[2 * t], X[2 * t + 1]);
    unsigned old = 0u;
    if (lane < 4) { lds[L_MX + w * 4 + lane] = m; lds[L_LS + w * 4 + lane] = l; }
    if (lane == 0) old = __hip_atomic_fetch_add((unsigned*)(lds + L_CNT), 1u, __ATOMIC_ACQ_REL, __HIP_MEMORY_SCOPE_WORKGROUP);
    if (__builtin_amdgcn_readfirstlane(old) == NWAVE - 1) {
      const float mw = lds[L_MX + (lane & 31)], lw = lds[L_LS + (lane & 31)];
      float M = fmaxf(mw, dpp<ROR4>(mw)); M = fmaxf(M, dpp<ROR8>(M)); M = swap16_max(M);
      const float sc = __builtin_amdgcn_exp2f((mw - M) * LOG2E);
      float L = lw * sc;
      L += dpp<ROR4>(L); L += dpp<ROR8>(L); L = swap16_add(L, L);
      if (lane < 32) lds[L_SC + lh * 8 + (lane >> 2)] = sc;
      if (lane < 4) put2(rw, lane * 512, (G_ML + g * 256 + s * 2) * 8, M, L, tag);
    }
  }
  const int hp = s & 1, cbk = (s >> 1) & 7;
  const rsrc_t ro = __builtin_amdgcn_make_buffer_rsrc((void*)(wo + (size_t)(512 * g + 256 * hp + 64 * (w & 3)) * D + 64 * cbk), 0, par ? 0 : 63 * 1024 + 128, 0x00020000);
  v4u wov[8], w1v, b1v, w2v;
  const unsigned xt = NTL((const unsigned*)(x + 128 * (w & 3) + 2 * lane));
  const u16 b216 = NTL(b2);

  unsigned bm[4];
#pragma unroll
  for (int mm = 0; mm < 4; ++mm) bm[mm] = bank == mm ? 0xffffffffu : 0u;
  f32x4 acc[8];
#pragma unroll
  for (int j = 0; j < 8; ++j) acc[j] = f32x4{0.f, 0.f, 0.f, 0.f};
  __builtin_amdgcn_sched_barrier(0);
  Loop<0, NLK / 8>::run([&](auto TT) {
    constexpr int T = TT.value;
    constexpr int base = NLK + 8 * T;
    v4u pa;
#pragma unroll
    for (int mm = 0; mm < 4; ++mm) pa[mm] = PK[T] & bm[mm];
#pragma unroll
    for (int jj = 0; jj < 4; ++jj) {
      v4u b0, b1;
#pragma unroll
      for (int mm = 0; mm < 4; ++mm) {
        b0[mm] = __builtin_amdgcn_perm(buf[base + 4 + mm][jj], buf[base + mm][jj], 0x05040100u);
        b1[mm] = __builtin_amdgcn_perm(buf[base + 4 + mm][jj], buf[base + mm][jj], 0x07060302u);
      }
      acc[2 * jj] = mfma(pa, b0, acc[2 * jj]);
      acc[2 * jj + 1] = mfma(pa, b1, acc[2 * jj + 1]);
    }
    __builtin_amdgcn_sched_barrier(0);
    Loop<0, 8>::run([&](auto I) {
      constexpr int idx = base + I.value;
      if constexpr (idx + WV < NL) buf[idx + WV] = ldkv<idx + WV>(rk, rv, koff, voff);
    });
    if constexpr (base + 8 + WV == NL + 8) {
      if (w < 4) {
#pragma unroll
        for (int i = 0; i < 8; ++i) wov[i] = __builtin_amdgcn_raw_buffer_load_b128(ro, (lane >> 3) * 8192 + (lane & 7) * 16, i * 1024, WAUX);
        const int fi = tid < FF / 8 ? tid : 0;
        w1v = NTL((const v4u*)(w1 + 8 * fi)); b1v = NTL((const v4u*)(b1 + 8 * fi)); w2v = NTL((const v4u*)(w2 + 8 * fi));
      }
    }
    __builtin_amdgcn_sched_barrier(0);
  });
  STAMP(5);
  WSTAMP(2);
  STAMPW(11, 7);
  {
    float r2[8];
#pragma unroll
    for (int j = 0; j < 8; ++j) r2[j] = swap16_add(swap32_add(acc[j][0], acc[j][2]), swap32_add(acc[j][1], acc[j][3]));
    *(f32x4*)(lds + L_PART + w * 512 + lane * 8) = f32x4{r2[0], r2[1], r2[2], r2[3]};
    *(f32x4*)(lds + L_PART + w * 512 + lane * 8 + 4) = f32x4{r2[4], r2[5], r2[6], r2[7]};
  }
  if (lane == 0) __hip_atomic_fetch_add((unsigned*)(lds + L_CNT) + 2, 1u, __ATOMIC_RELAXED, __HIP_MEMORY_SCOPE_WORKGROUP);
  f32x2 w1f[4], b1f[4];
  v4u wb[8];
  f32x4 wt[4];
  bool have_wt = false;
  auto mld = [&](auto) { return get2(rw, (lane & 31) * 16, (G_ML + (g * 4 + 2 * hp + ((w >> 1) & 1)) * 64) * 8); };
  auto weights = [&](v4u mv) {
    const float mp = asf(mv[1]);
    float M = fmaxf(mp, dpp<XOR1>(mp)); M = fmaxf(M, dpp<XOR2>(M)); M = fmaxf(M, dpp<ROR4>(M)); M = fmaxf(M, dpp<ROR8>(M)); M = swap16_max(M);
    const float e = __builtin_amdgcn_exp2f((mp - M) * LOG2E);
    float L = e * asf(mv[3]);
    L += dpp<XOR1>(L); L += dpp<XOR2>(L); L += dpp<ROR4>(L); L += dpp<ROR8>(L); L = swap16_add(L, L);
    lds[L_WT + w * 32 + (lane & 31)] = e * __builtin_amdgcn_rcpf(L);
#pragma unroll
    for (int i = 0; i < 4; ++i) wt[i] = *(const f32x4*)(lds + L_WT + w * 32 + 16 * (lane >> 5) + 4 * i);
#pragma unroll
    for (int i = 0; i < 4; ++i) asm volatile("" : "+v"(wt[i]));
  };
  if (w < 4) {
#pragma unroll
    for (int i = 0; i < 4; ++i) { w1f[i] = unpk(w1v[i]); b1f[i] = unpk(b1v[i]); }
    if (tid >= FF / 8) w2v = v4u{0u, 0u, 0u, 0u};
#pragma unroll
    for (int i = 0; i < 4; ++i) asm volatile("" : "+v"(w1f[i]), "+v"(b1f[i]));
    asm volatile("" : "+v"(w2v));
    if (par == 0) {
#pragma unroll
      for (int jj = 0; jj < 4; ++jj)
#pragma unroll
        for (int mm = 0; mm < 4; ++mm) {
          wb[2 * jj][mm] = __builtin_amdgcn_perm(wov[2 * mm + 1][jj], wov[2 * mm][jj], 0x05040100u);
          wb[2 * jj + 1][mm] = __builtin_amdgcn_perm(wov[2 * mm + 1][jj], wov[2 * mm][jj], 0x07060302u);
        }
#pragma unroll
      for (int i = 0; i < 8; ++i) asm volatile("" : "+v"(wb[i]));
      unsigned done = 0u;
#pragma unroll 1
      for (int spin = 0; spin < (SPIN_CAP << 4) && (done = __builtin_amdgcn_readfirstlane(cnt[2])) < GATE1; ++spin) __builtin_amdgcn_s_sleep(8);
      if (done < NWAVE - 1) {
        const v4u mv = mld(0);
        if (!any(stale(mv, tag))) { weights(mv); have_wt = true; }
      }
    }
  }
  WSTAMP(3);
  __syncthreads();
  if (w < 4) {
    const f32x4 sa = *(const f32x4*)(lds + L_SC + w * 8), sb = *(const f32x4*)(lds + L_SC + w * 8 + 4);
    f32x2 o0 = *(const f32x2*)(lds + L_PART + 2 * tid) * sa[0], o1 = *(const f32x2*)(lds + L_PART + 4 * 512 + 2 * tid) * sb[0];
#pragma unroll
    for (int i = 1; i < 4; ++i) {
      o0 += *(const f32x2*)(lds + L_PART + i * 512 + 2 * tid) * sa[i];
      o1 += *(const f32x2*)(lds + L_PART + (4 + i) * 512 + 2 * tid) * sb[i];
    }
    o0 += o1;
    put2(rw, (tid >> 5) * 16384 + (tid & 31) * 16, (G_ACC + g * 32 * 512 + (2 * (s & 15) + par) * 64) * 8, o0[0], o0[1], tag);
  }
  STAMP(6);
  if (par == 0) {
    __builtin_amdgcn_s_barrier();
    if (w < 4) {
      if (!have_wt) {
        v4u mv[1];
        gather<1>(mv, tag, mld, [](unsigned) { __builtin_amdgcn_s_sleep(8); });
        weights(mv[0]);
      }
      v4u av[16];
      gather16<SLP1>(av, rw, voff, (G_ACC + (g * 8 + hp * 4 + w) * 2048) * 8, tag);
      float oa[2] = {0.f, 0.f}, ob[2] = {0.f, 0.f};
#pragma unroll
      for (int i = 0; i < 16; ++i) { oa[i & 1] = fmaf(asf(av[i][1]), wt[i >> 2][i & 3], oa[i & 1]); ob[i & 1] = fmaf(asf(av[i][3]), wt[i >> 2][i & 3], ob[i & 1]); }
      const float o = swap32_add(oa[0] + oa[1], ob[0] + ob[1]);
      u16* obp = (u16*)(lds + L_OB) + 64 * w;
      obp[2 * (lane & 31) + (lane >> 5)] = (u16)cvtpk(o, o);
      STAMP(7);
      const v4u af = *(const v4u*)(obp + 16 * lg + 8 * (lane & 1));
      float z[8];
#pragma unroll
      for (int j = 0; j < 8; ++j) { const f32x4 d = mfma(af, wb[j], f32x4{0.f, 0.f, 0.f, 0.f}); z[j] = bsel<0xC>(d[0], d[1]); }
      if (lane < 16) {
        *(f32x4*)(lds + L_RED + w * 128 + lane * 8) = f32x4{z[0], z[1], z[2], z[3]};
        *(f32x4*)(lds + L_RED + w * 128 + lane * 8 + 4) = f32x4{z[4], z[5], z[6], z[7]};
      }
    }
    __syncthreads();
    if (tid < 32) {
      f32x2 t[8];
#pragma unroll
      for (int i = 0; i < 8; ++i) t[i] = *(const f32x2*)(lds + L_RED + i * 64 + 2 * lane);
      const f32x2 zz = ((t[0] + t[1]) + (t[2] + t[3])) + ((t[4] + t[5]) + (t[6] + t[7]));
      put2(rw, voff, (G_Z + ((cbk >> 1) * 16 + 2 * g + hp) * 128 + (cbk & 1) * 64) * 8, zz[0], zz[1], tag);
    }
  }
  __builtin_amdgcn_s_barrier();
  STAMP(8);
  if (w >= 4) {
    v4u zv[16];
    if (SLPZ && par) __builtin_amdgcn_s_sleep(SLPZ);
    gather16<SLP2>(zv, rw, voff, (G_Z + (w & 3) * 2048) * 8, tag);
    float ta[8], tb[8];
#pragma unroll
    for (int i = 0; i < 8; ++i) { ta[i] = asf(zv[2 * i][1]) + asf(zv[2 * i + 1][1]); tb[i] = asf(zv[2 * i][3]) + asf(zv[2 * i + 1][3]); }
    const f32x2 t = {((ta[0] + ta[1]) + (ta[2] + ta[3])) + ((ta[4] + ta[5]) + (ta[6] + ta[7])), ((tb[0] + tb[1]) + (tb[2] + tb[3])) + ((tb[4] + tb[5]) + (tb[6] + tb[7]))};
    *(f32x2*)(lds + L_H + 128 * (w & 3) + 2 * lane) = rbf2(unpk(xt) + rbf2(t));
  }
  __syncthreads();
  STAMP(9);
  if (w >= 4) return;
  {
    const f32x4 ha = *(const f32x4*)(lds + L_H + 8 * lane), hb = *(const f32x4*)(lds + L_H + 8 * lane + 4);
    const int c0 = 2 * blockIdx.x;
    const f32x2 hc = *(const f32x2*)(lds + L_H + c0);
    const f32x2 v[4] = {f32x2{ha[0], ha[1]}, f32x2{ha[2], ha[3]}, f32x2{hb[0], hb[1]}, f32x2{hb[2], hb[3]}};
    float mb1, rs1, mb2, rs2;
    ln_stats(v, mb1, rs1);
    unsigned y1[4];
#pragma unroll
    for (int i = 0; i < 4; ++i) y1[i] = ln_apply_pk(v[i], mb1, rs1);
    const f32x2 n1 = rbf2(rbf2(hc - mb1) * rs1);
    ln_finish(dot2(y1[0], 0x3f803f80u, dot2(y1[1], 0x3f803f80u, 0.f)) + dot2(y1[2], 0x3f803f80u, dot2(y1[3], 0x3f803f80u, 0.f)),
              dot2(y1[0], y1[0], dot2(y1[1], y1[1], 0.f)) + dot2(y1[2], y1[2], dot2(y1[3], y1[3], 0.f)), 0.f, mb2, rs2);
    const f32x2 n = rbf2(rbf2(n1 - mb2) * rs2);
    constexpr float K1 = -2.f * LOG2E * 0.7978845608028654f, K2 = K1 * 0.044715f;
    float y[2] = {0.f, 0.f};
#pragma unroll
    for (int i = 0; i < 4; ++i)
#pragma unroll
      for (int c = 0; c < 2; ++c) {
        const f32x2 u = rbf2(rbf2(w1f[i] * n[c]) + b1f[i]);
        const f32x2 a = u * (u * u * K2 + K1);
        const f32x2 e = f32x2{__builtin_amdgcn_exp2f(a[0]), __builtin_amdgcn_exp2f(a[1])} + 1.f;
        const f32x2 gl = u * f32x2{__builtin_amdgcn_rcpf(e[0]), __builtin_amdgcn_rcpf(e[1])};
        y[c] = dot2(cvtpk(gl[0], gl[1]), w2v[i], y[c]);
      }
    wave_sum2_l63(y[0], y[1]);
    if (lane == 63) *(f32x2*)(lds + L_RED + 2 * w) = f32x2{y[0], y[1]};
    __syncthreads();
    if (tid == 0) {
      const f32x4 ta = *(const f32x4*)(lds + L_RED), tb = *(const f32x4*)(lds + L_RED + 4);
      const f32x2 ysum = (f32x2{ta[0], ta[1]} + f32x2{ta[2], ta[3]}) + (f32x2{tb[0], tb[1]} + f32x2{tb[2], tb[3]});
      const f32x2 r = hc + rbf2(rbf2(ysum) + bf2f(b216));
      *(unsigned*)(out + c0) = cvtpk(r[0], r[1]);
      if (blockIdx.x == 0) __hip_atomic_store(ws + G_EPOCH, ((u64)cnt[5] << 32) | tag, RLX_AGENT);
    }
  }
  STAMP(10);
}

extern "C" void kernel_launch(void* const* d_in, const int* in_sizes, int n_in, void* d_out, int out_size, void* d_ws, size_t ws_size,
                              hipStream_t stream) {
  k_fused<<<NWG, NTHR, 0, stream>>>((const u16*)d_in[0], (const u16*)d_in[1], (const u16*)d_in[2], (const u16*)d_in[3], (u64*)d_ws, (const u16*)d_in[4],
                                    (const u16*)d_in[6], (const u16*)d_in[7], (const u16*)d_in[8], (const u16*)d_in[9], (u16*)d_out);
}
```

```cpp
#define PROBE 99
#include <hip/hip_runtime.h>
#include <hip/hip_bf16.h>
#include <cstdint>
#include <type_traits>

#define KAUX 2
#define VAUX 2
#define WAUX 2
#define W0 32
#define WV 32
#define THR 40
#define TILES_EVEN 68
#define SLPL 32
#define SLPQ 0
#define SLPZ 0
#define ORDB 0
#define GATE1 5
#define MANY1 8
#define MANY2 12
#define SLP1 1
#define SLP2 1
#define CODEPF 0
#define PAT 0
#define UNTHR 1
#define DIAG 0

typedef unsigned v4u __attribute__((ext_vector_type(4)));
typedef unsigned v2u __attribute__((ext_vector_type(2)));
typedef float f32x4 __attribute__((ext_vector_type(4)));
typedef float f32x2 __attribute__((ext_vector_type(2)));
typedef __bf16 bf16x8 __attribute__((ext_vector_type(8)));
typedef __bf16 bf16x2 __attribute__((ext_vector_type(2)));
typedef unsigned short u16;
typedef unsigned long long u64;
typedef __amdgpu_buffer_rsrc_t rsrc_t;
#define GAS __attribute__((address_space(1)))
#define LAS __attribute__((address_space(3)))
typedef GAS u64 gu64;
#define RLX_AGENT __ATOMIC_RELAXED, __HIP_MEMORY_SCOPE_AGENT

constexpr int D = 512, SK = 65536, DH = 128, FF = 1792;
constexpr int NSPLIT = 32, NWAVE = 8, NTHR = 512, NWG = 256;
constexpr int TE = TILES_EVEN, TO = 128 - TE;
constexpr int NTILE = (TE + 7) / 8;
constexpr int NLK = NTILE * 8;
constexpr int NL = 2 * NLK;
constexpr float EPS = 1e-5f;
constexpr float SCALE = 0.08838834764831845f;
constexpr float LOG2E = 1.4426950408889634f;
constexpr int SPIN_CAP = 1 << 19;

constexpr int G_EPOCH = 0;
constexpr int G_Q = 512;
constexpr int G_ML = G_Q + 8 * 4 * 512;
constexpr int G_Z = G_ML + 8 * 4 * 32 * 2;
constexpr int G_ACC = G_Z + 16 * 512;
constexpr int G_END = G_ACC + 256 * 512;
constexpr int G_DIAG = G_END;

__device__ __forceinline__ float bf2f(u16 b) { return __uint_as_float((unsigned)b << 16); }
__device__ __forceinline__ float rbf(float x) { return (float)(__bf16)x; }
__device__ __forceinline__ float asf(unsigned w) { return __uint_as_float(w); }
__device__ __forceinline__ unsigned cvtpk(float lo, float hi) {
  bf16x2 r = {(__bf16)lo, (__bf16)hi};
  return __builtin_bit_cast(unsigned, r);
}
__device__ __forceinline__ f32x2 unpk(unsigned w) { return f32x2{asf(w << 16), asf(w & 0xffff0000u)}; }
__device__ __forceinline__ f32x2 rbf2(f32x2 v) {
  const unsigned p = cvtpk(v[0], v[1]);
  return f32x2{asf(__builtin_amdgcn_perm(p, p, 0x01000c0cu)), asf(__builtin_amdgcn_perm(p, p, 0x03020c0cu))};
}
__device__ __forceinline__ float dot2(unsigned a, unsigned b, float c) {
  return __builtin_amdgcn_fdot2_f32_bf16(__builtin_bit_cast(bf16x2, a), __builtin_bit_cast(bf16x2, b), c, false);
}
__device__ __forceinline__ f32x4 mfma(v4u a, v4u b, f32x4 c) {
  return __builtin_amdgcn_mfma_f32_16x16x32_bf16(__builtin_bit_cast(bf16x8, a), __builtin_bit_cast(bf16x8, b), c, 0, 0, 0);
}
template <int CTRL> __device__ __forceinline__ float dpp(float x) {
  return __builtin_bit_cast(float, __builtin_amdgcn_mov_dpp(__builtin_bit_cast(int, x), CTRL, 0xf, 0xf, true));
}
template <int CTRL, int RM> __device__ __forceinline__ float dppz(float x) {
  return __builtin_bit_cast(float, __builtin_amdgcn_update_dpp(0, __builtin_bit_cast(int, x), CTRL, RM, 0xf, false));
}
constexpr int XOR1 = 0xB1, XOR2 = 0x4E, ROR8 = 0x128, ROR4 = 0x124, ROR12 = 0x12C, HMIR = 0x141, BC15 = 0x142, BC31 = 0x143;
__device__ __forceinline__ float swap32_add(float a, float b) {
  auto s = __builtin_amdgcn_permlane32_swap(__float_as_uint(a), __float_as_uint(b), false, false);
  return asf(s[0]) + asf(s[1]);
}
__device__ __forceinline__ float swap16_add(float a, float b) {
  auto s = __builtin_amdgcn_permlane16_swap(__float_as_uint(a), __float_as_uint(b), false, false);
  return asf(s[0]) + asf(s[1]);
}
__device__ __forceinline__ float swap16_max(float x) {
  auto s = __builtin_amdgcn_permlane16_swap(__float_as_uint(x), __float_as_uint(x), false, false);
  return fmaxf(asf(s[0]), asf(s[1]));
}
__device__ __forceinline__ float xrow_sum(float x) { x = swap16_add(x, x); return swap32_add(x, x); }
__device__ __forceinline__ float xrow_max(float x) {
  x = swap16_max(x);
  auto t = __builtin_amdgcn_permlane32_swap(__float_as_uint(x), __float_as_uint(x), false, false);
  return fmaxf(asf(t[0]), asf(t[1]));
}
__device__ __forceinline__ float rdlane(float x, int l) { return __builtin_bit_cast(float, __builtin_amdgcn_readlane(__builtin_bit_cast(int, x), l)); }
__device__ __forceinline__ void wave_sum2_l63(float& a, float& b) {
  a += dpp<XOR1>(a); b += dpp<XOR1>(b);
  a += dpp<XOR2>(a); b += dpp<XOR2>(b);
  a += dpp<ROR4>(a); b += dpp<ROR4>(b);
  a += dpp<ROR8>(a); b += dpp<ROR8>(b);
  a += dppz<BC15, 0xa>(a); b += dppz<BC15, 0xa>(b);
  a += dppz<BC31, 0xc>(a); b += dppz<BC31, 0xc>(b);
}
__device__ __forceinline__ void ln_finish(float s1, float s2, float c, float& mb, float& rs) {
  wave_sum2_l63(s1, s2);
  const float dm = rdlane(s1, 63) * (1.f / D);
  const float var = fmaxf(rdlane(s2, 63) * (1.f / D) - dm * dm, 0.f);
  const f32x2 mv = rbf2(f32x2{c + dm, var});
  mb = mv[0];
  rs = __builtin_amdgcn_rcpf(rbf(__builtin_amdgcn_sqrtf(rbf(mv[1] + EPS))));
}
__device__ __forceinline__ void ln_stats(const f32x2 (&v)[4], float& mb, float& rs) {
  const float c = rdlane(v[0][0], 0);
  const f32x2 d0 = v[0] - c, d1 = v[1] - c, d2 = v[2] - c, d3 = v[3] - c;
  const f32x2 s1 = (d0 + d1) + (d2 + d3), s2 = (d0 * d0 + d1 * d1) + (d2 * d2 + d3 * d3);
  ln_finish(s1[0] + s1[1], s2[0] + s2[1], c, mb, rs);
}
__device__ __forceinline__ unsigned ln_apply_pk(f32x2 v, float mb, float rs) { const f32x2 t = rbf2(v - mb) * rs; return cvtpk(t[0], t[1]); }

constexpr int AUX_SC1 = 16, AUX_NT = 2;
__device__ __forceinline__ v4u get2(rsrc_t rs, int voff, int soff) { return __builtin_amdgcn_raw_buffer_load_b128(rs, voff, soff, AUX_SC1); }
__device__ __forceinline__ void put2(rsrc_t rs, int voff, int soff, float a, float b, unsigned tag) {
  __builtin_amdgcn_raw_buffer_store_b128(v4u{tag, __float_as_uint(a), tag, __float_as_uint(b)}, rs, voff + soff, 0, AUX_SC1);
}
__device__ __forceinline__ unsigned stale(v4u g, unsigned tag) { return (g[0] ^ tag) | (g[2] ^ tag); }
__device__ __forceinline__ bool any(unsigned bad) {
  asm("" : "+v"(bad));
  return __builtin_amdgcn_ballot_w64(bad != 0u) != 0ull;
}
template <int I, int N> struct Loop {
  template <class F> static __device__ __forceinline__ void run(F&& f) {
    if constexpr (I < N) { f(std::integral_constant<int, I>{}); Loop<I + 1, N>::run(f); }
  }
};
template <int N, class LD, class NAP> __device__ __forceinline__ void gather(v4u (&g)[N], unsigned tag, LD&& ld, NAP&& nap) {
  unsigned pend = (1u << N) - 1u;
#pragma unroll 1
  for (int spin = 0; spin < SPIN_CAP; ++spin) {
    asm volatile("" ::: "memory");
    Loop<0, N>::run([&](auto I) { if (pend & (1u << I.value)) g[I.value] = ld(I); });
    Loop<0, N>::run([&](auto I) { if (pend & (1u << I.value)) { if (!any(stale(g[I.value], tag))) pend &= ~(1u << I.value); } });
    if (pend == 0u) break;
    nap(pend);
  }
}
#define G16_LD(S, J) ".set gi_, " #J "\n\t.rept 4\n\tbuffer_load_dwordx4 v[128+4*gi_:131+4*gi_], %[vo], %[rs], " S " offen offset:(gi_&3)*1024 sc1\n\t.set gi_, gi_+1\n\t.endr\n\t"
#define G16_RE(S, J) ".set gi_, " #J "\n\t.rept 4\n\tv_cmpx_ne_u32_e32 vcc, 0, v[108+gi_]\n\tbuffer_load_dwordx4 v[128+4*gi_:131+4*gi_], %[vo], %[rs], " S " offen offset:(gi_&3)*1024 sc1\n\ts_mov_b64 exec, -1\n\t.set gi_, gi_+1\n\t.endr\n\t"
template <int SLP> __device__ __forceinline__ void gather16(v4u (&g)[16], rsrc_t rs, int voff, int soff, unsigned tag) {
  unsigned spin = SPIN_CAP;
  const int s0 = __builtin_amdgcn_readfirstlane(soff);
  asm volatile(
      "s_nop 4\n\t"
      G16_LD("%[s0]", 0) G16_LD("%[s1]", 4) G16_LD("%[s2]", 8) G16_LD("%[s3]", 12)
      "1:\n\t"
      "s_waitcnt vmcnt(0)\n\t"
      ".set gi_, 0\n\t.rept 16\n\tv_bitop3_b32 v[108+gi_], v[128+4*gi_], v[130+4*gi_], %[tg] bitop3:0x7e\n\t.set gi_, gi_+1\n\t.endr\n\t"
      "v_or3_b32 v124, v108, v109, v110\n\t"
      ".set gi_, 3\n\t.rept 6\n\tv_or3_b32 v124, v124, v[108+gi_], v[109+gi_]\n\t.set gi_, gi_+2\n\t.endr\n\t"
      "v_or_b32_e32 v124, v124, v123\n\t"
      "v_cmp_ne_u32_e32 vcc, 0, v124\n\t"
      "s_cbranch_vccz 2f\n\t"
      "s_sub_u32 %[sp], %[sp], 1\n\t"
      "s_cbranch_scc1 2f\n\t"
      "s_sleep %[sl]\n\t"
      G16_RE("%[s0]", 0) G16_RE("%[s1]", 4) G16_RE("%[s2]", 8) G16_RE("%[s3]", 12)
      "s_branch 1b\n\t"
      "2:"
      : "={v[128:131]}"(g[0]), "={v[132:135]}"(g[1]), "={v[136:139]}"(g[2]), "={v[140:143]}"(g[3]), "={v[144:147]}"(g[4]), "={v[148:151]}"(g[5]),
        "={v[152:155]}"(g[6]), "={v[156:159]}"(g[7]), "={v[160:163]}"(g[8]), "={v[164:167]}"(g[9]), "={v[168:171]}"(g[10]), "={v[172:175]}"(g[11]),
        "={v[176:179]}"(g[12]), "={v[180:183]}"(g[13]), "={v[184:187]}"(g[14]), "={v[188:191]}"(g[15]), [sp] "+s"(spin)
      : [vo] "{v125}"(voff), [rs] "s"(rs), [s0] "s"(s0), [s1] "s"(s0 + 4096), [s2] "s"(s0 + 8192), [s3] "s"(s0 + 12288), [tg] "s"(tag), [sl] "i"(SLP)
      : "v108", "v109", "v110", "v111", "v112", "v113", "v114", "v115", "v116", "v117", "v118", "v119", "v120", "v121", "v122", "v123", "v124",
        "vcc", "scc", "memory");
}
__device__ __forceinline__ u64 rt() { return __builtin_amdgcn_s_memrealtime(); }

constexpr int BLK_STRIDE = 4096;
template <int I> __device__ __forceinline__ v4u ldkv(rsrc_t rk, rsrc_t rv, int koff, int voff) {
  if constexpr (I < NLK) return __builtin_amdgcn_raw_buffer_load_b128(rk, koff + (I & 3) * 1024, (I >> 2) * BLK_STRIDE, KAUX);
  else return __builtin_amdgcn_raw_buffer_load_b128(rv, voff + ((I - NLK) & 3) * 1024, ((I - NLK) >> 2) * BLK_STRIDE, VAUX);
}
template <int BM> __device__ __forceinline__ float bsel(float old, float src) {
  return __builtin_bit_cast(float, __builtin_amdgcn_update_dpp(__builtin_bit_cast(int, old), __builtin_bit_cast(int, src), 0xE4, 0xf, BM, false));
}
#define STR2(x) #x
#define STR(x) STR2(x)
#define WSTAMP(k) do { } while (0)
#define STAMPW(k, ww) do { } while (0)
#define STAMP(k) do { if ((k) == PROBE) tprobe = rt(); if ((k) == 10) { while (rt() - tprobe < 10000ull) __builtin_amdgcn_s_sleep(8); } } while (0)
#define NTL(p) __builtin_nontemporal_load(p)

constexpr int L_PART = 0;
constexpr int L_RED = L_PART + 8 * 512;
constexpr int L_WT = L_RED + 8 * 64;
constexpr int L_OB = L_WT + 4 * 32;
constexpr int L_MX = L_WT + 8 * 32;
constexpr int L_LS = L_MX + 32;
constexpr int L_SC = L_LS + 32;
constexpr int L_CNT = L_SC + 32;
constexpr int L_Q = L_CNT + 8;
constexpr int L_H = L_Q + 256;
constexpr int L_END = L_H + 512;

__global__ void __launch_bounds__(NTHR, 2) k_fused(const u16* __restrict__ x, const u16* __restrict__ kc, const u16* __restrict__ vc,
                                                   const u16* __restrict__ wq, u64* __restrict__ wsp, const u16* __restrict__ wo,
                                                   const u16* __restrict__ w1, const u16* __restrict__ b1, const u16* __restrict__ w2,
                                                   const u16* __restrict__ b2, u16* __restrict__ out) {
  __shared__ __attribute__((aligned(16))) float lds[L_END];
  u64 tfirst;
  asm volatile("s_memrealtime %0\n\ts_waitcnt lgkmcnt(0)" : "=s"(tfirst) :: "memory");
  __builtin_amdgcn_sched_barrier(0);
  gu64* ws = (gu64*)wsp;
  const int tid = threadIdx.x, lane = tid & 63, w = __builtin_amdgcn_readfirstlane(tid >> 6);
  const int par = blockIdx.x & 1, jx = blockIdx.x >> 3;
  const int g = (blockIdx.x & 6) + (jx >> 4), s = 16 * par + (jx & 15);
  u64 tprobe = tfirst;
  STAMP(0);
  volatile LAS unsigned* cnt = (volatile LAS unsigned*)(lds + L_CNT);
  if (tid < 4) cnt[tid] = 0u;
  asm volatile("s_waitcnt lgkmcnt(0)\n\ts_barrier" :: "s"(x), "s"(kc), "s"(vc), "s"(wq), "s"(wsp) : "memory");
  unsigned tag;
  const rsrc_t rw = __builtin_amdgcn_make_buffer_rsrc((void*)wsp, 0, G_END * 8, 0x00020000);
  const int ntw = par ? TO : TE, tlo = (ntw * w) >> 3, nt = ((ntw * (w + 1)) >> 3) - tlo;
  const size_t key0 = (size_t)g * SK + 32 * ((par ? 16 * TE : 0) + (jx & 15) * ntw + tlo);
  const int lg = lane >> 4, bank = (lane >> 2) & 3, lh = lane & 3;
  const rsrc_t rk = __builtin_amdgcn_make_buffer_rsrc((void*)(kc + key0 * DH), 0, nt * 8192, 0x00020000);
  const rsrc_t rv = __builtin_amdgcn_make_buffer_rsrc((void*)(vc + key0 * DH), 0, nt * 8192, 0x00020000);
  const int koff = 16 * (16 * bank + 4 * lg + lh), voff = 16 * lane;
  v4u buf[NL];
  if (__builtin_expect(w == 0, 1)) {
    __builtin_amdgcn_s_setprio(3);
    const int kq = s >> 3, cq = 64 * (s & 7);
    const rsrc_t rq = __builtin_amdgcn_make_buffer_rsrc((void*)(wq + (size_t)kq * 128 * 4096 + 512 * g + cq), 0, 127 * 8192 + 128, 0x00020000);
    const v4u xv = NTL((const v4u*)(x + 8 * lane));
    const v4u xa = NTL((const v4u*)(x + 128 * kq + 16 * (lane >> 3))), xb = NTL((const v4u*)(x + 128 * kq + 16 * (lane >> 3) + 8));
    v4u wv[16];
    const int qoff = (lane >> 3) * (16 * 8192) + (lane & 7) * 16;
#pragma unroll
    for (int i = 0; i < 16; ++i) wv[i] = __builtin_amdgcn_raw_buffer_load_b128(rq, qoff, i * 8192, WAUX);
    const u64 epoch = *(const __attribute__((address_space(4))) u64*)(wsp + G_EPOCH);
    __builtin_amdgcn_sched_barrier(0);
    if (ORDB) asm volatile("s_barrier" ::: "memory");
    f32x2 v[4];
#pragma unroll
    for (int i = 0; i < 4; ++i) v[i] = unpk(xv[i]);
    float mb, rs;
    ln_stats(v, mb, rs);
    unsigned xn[8];
#pragma unroll
    for (int i = 0; i < 8; ++i) xn[i] = ln_apply_pk(unpk(i < 4 ? xa[i & 3] : xb[i & 3]), mb, rs);
    {
      u64 mixv = epoch + 0x9E3779B97F4A7C15ull;
      mixv = (mixv ^ (mixv >> 30)) * 0xBF58476D1CE4E5B9ull;
      mixv = (mixv ^ (mixv >> 27)) * 0x94D049BB133111EBull;
      mixv ^= mixv >> 31;
      const unsigned lo = (unsigned)mixv;
      tag = __builtin_amdgcn_readfirstlane(lo == (unsigned)epoch ? lo ^ 1u : lo);
      cnt[4] = tag;
      cnt[5] = (unsigned)(mixv >> 32);
    }
    float a[8] = {0.f, 0.f, 0.f, 0.f, 0.f, 0.f, 0.f, 0.f};
#pragma unroll
    for (int j = 0; j < 8; ++j)
#pragma unroll
      for (int d = 0; d < 4; ++d) {
        a[2 * d] = dot2(__builtin_amdgcn_perm(wv[2 * j + 1][d], wv[2 * j][d], 0x05040100u), xn[j], a[2 * d]);
        a[2 * d + 1] = dot2(__builtin_amdgcn_perm(wv[2 * j + 1][d], wv[2 * j][d], 0x07060302u), xn[j], a[2 * d + 1]);
      }
    float c0 = swap16_add(swap32_add(a[0], a[4]), swap32_add(a[2], a[6])), c1 = swap16_add(swap32_add(a[1], a[5]), swap32_add(a[3], a[7]));
    c0 += dpp<ROR8>(c0); c1 += dpp<ROR8>(c1);
    if ((lane & 8) == 0) put2(rw, (lane & 7) * 64 + (lane >> 4) * 16, (G_Q + (g * 4 + kq) * 512 + cq) * 8, c0, c1, tag);
    STAMP(1);
    v4u qv[16];
    gather16<SLPQ>(qv, rw, voff, (G_Q + g * 2048) * 8, tag);
#pragma unroll
    for (int i = 0; i < 4; ++i)
      ((unsigned*)(lds + L_Q))[64 * i + lane] = cvtpk((asf(qv[i][1]) + asf(qv[4 + i][1])) + (asf(qv[8 + i][1]) + asf(qv[12 + i][1])),
                                                      (asf(qv[i][3]) + asf(qv[4 + i][3])) + (asf(qv[8 + i][3]) + asf(qv[12 + i][3])));
    asm volatile("s_waitcnt lgkmcnt(0)" ::: "memory");
    cnt[1] = 1u;
    __builtin_amdgcn_s_setprio(0);
    STAMP(2);
    WSTAMP(0);
    Loop<0, W0>::run([&](auto I) { buf[I.value] = ldkv<I.value>(rk, rv, koff, voff); });
  } else {
    if (ORDB) asm volatile("s_barrier" ::: "memory");
    Loop<0, W0>::run([&](auto I) { buf[I.value] = ldkv<I.value>(rk, rv, koff, voff); });
    WSTAMP(0);
#pragma unroll 1
    for (int spin = 0; spin < (SPIN_CAP << 4) && __builtin_amdgcn_readfirstlane(cnt[1]) == 0u; ++spin) __builtin_amdgcn_s_sleep(2);
    asm volatile("" ::: "memory");
    tag = __builtin_amdgcn_readfirstlane(cnt[4]);
  }
  STAMPW(3, 1);
  const v4u qb = *(const v4u*)((const u16*)(lds + L_Q) + lh * DH + 8 * (4 * lg + bank));
  __builtin_amdgcn_sched_barrier(0);

  float X[NLK / 4];
  Loop<0, NLK / 4>::run([&](auto T) {
    constexpr int t = T.value;
    f32x4 d[4];
    Loop<0, 4>::run([&](auto M) {
      constexpr int i = 4 * t + M.value;
      d[M.value] = mfma(buf[i], qb, f32x4{0.f, 0.f, 0.f, 0.f});
      if constexpr (i + W0 < NLK + WV) buf[i + W0] = ldkv<i + W0>(rk, rv, koff, voff);
    });
    float R[4];
#pragma unroll
    for (int k = 0; k < 4; ++k) R[k] = bsel<8>(bsel<4>(bsel<2>(d[k][0], d[(k + 1) & 3][1]), d[(k + 2) & 3][2]), d[(k + 3) & 3][3]);
    X[t] = ((R[0] + dpp<ROR4>(R[1])) + dpp<ROR8>(R[2])) + dpp<ROR12>(R[3]);
    asm volatile("" : "+v"(X[t]));
    __builtin_amdgcn_sched_barrier(0);
  });
  STAMP(4);
  WSTAMP(1);
  unsigned PK[NLK / 8];
  {
    float m = -INFINITY;
#pragma unroll
    for (int t = 0; t < NLK / 8; ++t) {
      f32x2 sc = rbf2(rbf2(f32x2{X[2 * t], X[2 * t + 1]}) * SCALE);
      if (t >= TO / 8 && t >= nt) sc = f32x2{-INFINITY, -INFINITY};
      X[2 * t] = sc[0]; X[2 * t + 1] = sc[1];
      m = fmaxf(m, fmaxf(sc[0], sc[1]));
    }
    m = fmaxf(m, dpp<ROR4>(m)); m = fmaxf(m, dpp<ROR8>(m)); m = xrow_max(m);
    float l = 0.f;
#pragma unroll
    for (int t = 0; t < NLK / 4; ++t) { X[t] = __builtin_amdgcn_exp2f((X[t] - m) * LOG2E); l += X[t]; }
    l += dpp<ROR4>(l); l += dpp<ROR8>(l); l = xrow_sum(l);
#pragma unroll
    for (int t = 0; t < NLK / 8; ++t) PK[t] = cvtpk(X[2 * t], X[2 * t + 1]);
    unsigned old = 0u;
    if (lane < 4) { lds[L_MX + w * 4 + lane] = m; lds[L_LS + w * 4 + lane] = l; }
    if (lane == 0) old = __hip_atomic_fetch_add((unsigned*)(lds + L_CNT), 1u, __ATOMIC_ACQ_REL, __HIP_MEMORY_SCOPE_WORKGROUP);
    if (__builtin_amdgcn_readfirstlane(old) == NWAVE - 1) {
      const float mw = lds[L_MX + (lane & 31)], lw = lds[L_LS + (lane & 31)];
      float M = fmaxf(mw, dpp<ROR4>(mw)); M = fmaxf(M, dpp<ROR8>(M)); M = swap16_max(M);
      const float sc = __builtin_amdgcn_exp2f((mw - M) * LOG2E);
      float L = lw * sc;
      L += dpp<ROR4>(L); L += dpp<ROR8>(L); L = swap16_add(L, L);
      if (lane < 32) lds[L_SC + lh * 8 + (lane >> 2)] = sc;
      if (lane < 4) put2(rw, lane * 512, (G_ML + g * 256 + s * 2) * 8, M, L, tag);
    }
  }
  const int hp = s & 1, cbk = (s >> 1) & 7;
  const rsrc_t ro = __builtin_amdgcn_make_buffer_rsrc((void*)(wo + (size_t)(512 * g + 256 * hp + 64 * (w & 3)) * D + 64 * cbk), 0, par ? 0 : 63 * 1024 + 128, 0x00020000);
  v4u wov[8], w1v, b1v, w2v;
  const unsigned xt = NTL((const unsigned*)(x + 128 * (w & 3) + 2 * lane));
  const u16 b216 = NTL(b2);

  unsigned bm[4];
#pragma unroll
  for (int mm = 0; mm < 4; ++mm) bm[mm] = bank == mm ? 0xffffffffu : 0u;
  f32x4 acc[8];
#pragma unroll
  for (int j = 0; j < 8; ++j) acc[j] = f32x4{0.f, 0.f, 0.f, 0.f};
  __builtin_amdgcn_sched_barrier(0);
  Loop<0, NLK / 8>::run([&](auto TT) {
    constexpr int T = TT.value;
    constexpr int base = NLK + 8 * T;
    v4u pa;
#pragma unroll
    for (int mm = 0; mm < 4; ++mm) pa[mm] = PK[T] & bm[mm];
#pragma unroll
    for (int jj = 0; jj < 4; ++jj) {
      v4u b0, b1;
#pragma unroll
      for (int mm = 0; mm < 4; ++mm) {
        b0[mm] = __builtin_amdgcn_perm(buf[base + 4 + mm][jj], buf[base + mm][jj], 0x05040100u);
        b1[mm] = __builtin_amdgcn_perm(buf[base + 4 + mm][jj], buf[base + mm][jj], 0x07060302u);
      }
      acc[2 * jj] = mfma(pa, b0, acc[2 * jj]);
      acc[2 * jj + 1] = mfma(pa, b1, acc[2 * jj + 1]);
    }
    __builtin_amdgcn_sched_barrier(0);
    Loop<0, 8>::run([&](auto I) {
      constexpr int idx = base + I.value;
      if constexpr (idx + WV < NL) buf[idx + WV] = ldkv<idx + WV>(rk, rv, koff, voff);
    });
    if constexpr (base + 8 + WV == NL + 8) {
      if (w < 4) {
#pragma unroll
        for (int i = 0; i < 8; ++i) wov[i] = __builtin_amdgcn_raw_buffer_load_b128(ro, (lane >> 3) * 8192 + (lane & 7) * 16, i * 1024, WAUX);
        const int fi = tid < FF / 8 ? tid : 0;
        w1v = NTL((const v4u*)(w1 + 8 * fi)); b1v = NTL((const v4u*)(b1 + 8 * fi)); w2v = NTL((const v4u*)(w2 + 8 * fi));
      }
    }
    __builtin_amdgcn_sched_barrier(0);
  });
  STAMP(5);
  WSTAMP(2);
  STAMPW(11, 7);
  {
    float r2[8];
#pragma unroll
    for (int j = 0; j < 8; ++j) r2[j] = swap16_add(swap32_add(acc[j][0], acc[j][2]), swap32_add(acc[j][1], acc[j][3]));
    *(f32x4*)(lds + L_PART + w * 512 + lane * 8) = f32x4{r2[0], r2[1], r2[2], r2[3]};
    *(f32x4*)(lds + L_PART + w * 512 + lane * 8 + 4) = f32x4{r2[4], r2[5], r2[6], r2[7]};
  }
  if (lane == 0) __hip_atomic_fetch_add((unsigned*)(lds + L_CNT) + 2, 1u, __ATOMIC_RELAXED, __HIP_MEMORY_SCOPE_WORKGROUP);
  f32x2 w1f[4], b1f[4];
  v4u wb[8];
  f32x4 wt[4];
  bool have_wt = false;
  auto mld = [&](auto) { return get2(rw, (lane & 31) * 16, (G_ML + (g * 4 + 2 * hp + ((w >> 1) & 1)) * 64) * 8); };
  auto weights = [&](v4u mv) {
    const float mp = asf(mv[1]);
    float M = fmaxf(mp, dpp<XOR1>(mp)); M = fmaxf(M, dpp<XOR2>(M)); M = fmaxf(M, dpp<ROR4>(M)); M = fmaxf(M, dpp<ROR8>(M)); M = swap16_max(M);
    const float e = __builtin_amdgcn_exp2f((mp - M) * LOG2E);
    float L = e * asf(mv[3]);
    L += dpp<XOR1>(L); L += dpp<XOR2>(L); L += dpp<ROR4>(L); L += dpp<ROR8>(L); L = swap16_add(L, L);
    lds[L_WT + w * 32 + (lane & 31)] = e * __builtin_amdgcn_rcpf(L);
#pragma unroll
    for (int i = 0; i < 4; ++i) wt[i] = *(const f32x4*)(lds + L_WT + w * 32 + 16 * (lane >> 5) + 4 * i);
#pragma unroll
    for (int i = 0; i < 4; ++i) asm volatile("" : "+v"(wt[i]));
  };
  if (w < 4) {
#pragma unroll
    for (int i = 0; i < 4; ++i) { w1f[i] = unpk(w1v[i]); b1f[i] = unpk(b1v[i]); }
    if (tid >= FF / 8) w2v = v4u{0u, 0u, 0u, 0u};
#pragma unroll
    for (int i = 0; i < 4; ++i) asm volatile("" : "+v"(w1f[i]), "+v"(b1f[i]));
    asm volatile("" : "+v"(w2v));
    if (par == 0) {
#pragma unroll
      for (int jj = 0; jj < 4; ++jj)
#pragma unroll
        for (int mm = 0; mm < 4; ++mm) {
          wb[2 * jj][mm] = __builtin_amdgcn_perm(wov[2 * mm + 1][jj], wov[2 * mm][jj], 0x05040100u);
          wb[2 * jj + 1][mm] = __builtin_amdgcn_perm(wov[2 * mm + 1][jj], wov[2 * mm][jj], 0x07060302u);
        }
#pragma unroll
      for (int i = 0; i < 8; ++i) asm volatile("" : "+v"(wb[i]));
      unsigned done = 0u;
#pragma unroll 1
      for (int spin = 0; spin < (SPIN_CAP << 4) && (done = __builtin_amdgcn_readfirstlane(cnt[2])) < GATE1; ++spin) __builtin_amdgcn_s_sleep(8);
      if (done < NWAVE - 1) {
        const v4u mv = mld(0);
        if (!any(stale(mv, tag))) { weights(mv); have_wt = true; }
      }
    }
  }
  WSTAMP(3);
  __syncthreads();
  if (w < 4) {
    const f32x4 sa = *(const f32x4*)(lds + L_SC + w * 8), sb = *(const f32x4*)(lds + L_SC + w * 8 + 4);
    f32x2 o0 = *(const f32x2*)(lds + L_PART + 2 * tid) * sa[0], o1 = *(const f32x2*)(lds + L_PART + 4 * 512 + 2 * tid) * sb[0];
#pragma unroll
    for (int i = 1; i < 4; ++i) {
      o0 += *(const f32x2*)(lds + L_PART + i * 512 + 2 * tid) * sa[i];
      o1 += *(const f32x2*)(lds + L_PART + (4 + i) * 512 + 2 * tid) * sb[i];
    }
    o0 += o1;
    put2(rw, (tid >> 5) * 16384 + (tid & 31) * 16, (G_ACC + g * 32 * 512 + (2 * (s & 15) + par) * 64) * 8, o0[0], o0[1], tag);
  }
  STAMP(6);
  if (par == 0) {
    __builtin_amdgcn_s_barrier();
    if (w < 4) {
      if (!have_wt) {
        v4u mv[1];
        gather<1>(mv, tag, mld, [](unsigned) { __builtin_amdgcn_s_sleep(8); });
        weights(mv[0]);
      }
      v4u av[16];
      gather16<SLP1>(av, rw, voff, (G_ACC + (g * 8 + hp * 4 + w) * 2048) * 8, tag);
      float oa[2] = {0.f, 0.f}, ob[2] = {0.f, 0.f};
#pragma unroll
      for (int i = 0; i < 16; ++i) { oa[i & 1] = fmaf(asf(av[i][1]), wt[i >> 2][i & 3], oa[i & 1]); ob[i & 1] = fmaf(asf(av[i][3]), wt[i >> 2][i & 3], ob[i & 1]); }
      const float o = swap32_add(oa[0] + oa[1], ob[0] + ob[1]);
      u16* obp = (u16*)(lds + L_OB) + 64 * w;
      obp[2 * (lane & 31) + (lane >> 5)] = (u16)cvtpk(o, o);
      STAMP(7);
      const v4u af = *(const v4u*)(obp + 16 * lg + 8 * (lane & 1));
      float z[8];
#pragma unroll
      for (int j = 0; j < 8; ++j) { const f32x4 d = mfma(af, wb[j], f32x4{0.f, 0.f, 0.f, 0.f}); z[j] = bsel<0xC>(d[0], d[1]); }
      if (lane < 16) {
        *(f32x4*)(lds + L_RED + w * 128 + lane * 8) = f32x4{z[0], z[1], z[2], z[3]};
        *(f32x4*)(lds + L_RED + w * 128 + lane * 8 + 4) = f32x4{z[4], z[5], z[6], z[7]};
      }
    }
    __syncthreads();
    if (tid < 32) {
      f32x2 t[8];
#pragma unroll
      for (int i = 0; i < 8; ++i) t[i] = *(const f32x2*)(lds + L_RED + i * 64 + 2 * lane);
      const f32x2 zz = ((t[0] + t[1]) + (t[2] + t[3])) + ((t[4] + t[5]) + (t[6] + t[7]));
      put2(rw, voff, (G_Z + ((cbk >> 1) * 16 + 2 * g + hp) * 128 + (cbk & 1) * 64) * 8, zz[0], zz[1], tag);
    }
  }
  __builtin_amdgcn_s_barrier();
  STAMP(8);
  if (w >= 4) {
    v4u zv[16];
    if (SLPZ && par) __builtin_amdgcn_s_sleep(SLPZ);
    gather16<SLP2>(zv, rw, voff, (G_Z + (w & 3) * 2048) * 8, tag);
    float ta[8], tb[8];
#pragma unroll
    for (int i = 0; i < 8; ++i) { ta[i] = asf(zv[2 * i][1]) + asf(zv[2 * i + 1][1]); tb[i] = asf(zv[2 * i][3]) + asf(zv[2 * i + 1][3]); }
    const f32x2 t = {((ta[0] + ta[1]) + (ta[2] + ta[3])) + ((ta[4] + ta[5]) + (ta[6] + ta[7])), ((tb[0] + tb[1]) + (tb[2] + tb[3])) + ((tb[4] + tb[5]) + (tb[6] + tb[7]))};
    *(f32x2*)(lds + L_H + 128 * (w & 3) + 2 * lane) = rbf2(unpk(xt) + rbf2(t));
  }
  __syncthreads();
  STAMP(9);
  if (w >= 4) return;
  {
    const f32x4 ha = *(const f32x4*)(lds + L_H + 8 * lane), hb = *(const f32x4*)(lds + L_H + 8 * lane + 4);
    const int c0 = 2 * blockIdx.x;
    const f32x2 hc = *(const f32x2*)(lds + L_H + c0);
    const f32x2 v[4] = {f32x2{ha[0], ha[1]}, f32x2{ha[2], ha[3]}, f32x2{hb[0], hb[1]}, f32x2{hb[2], hb[3]}};
    float mb1, rs1, mb2, rs2;
    ln_stats(v, mb1, rs1);
    unsigned y1[4];
#pragma unroll
    for (int i = 0; i < 4; ++i) y1[i] = ln_apply_pk(v[i], mb1, rs1);
    const f32x2 n1 = rbf2(rbf2(hc - mb1) * rs1);
    ln_finish(dot2(y1[0], 0x3f803f80u, dot2(y1[1], 0x3f803f80u, 0.f)) + dot2(y1[2], 0x3f803f80u, dot2(y1[3], 0x3f803f80u, 0.f)),
              dot2(y1[0], y1[0], dot2(y1[1], y1[1], 0.f)) + dot2(y1[2], y1[2], dot2(y1[3], y1[3], 0.f)), 0.f, mb2, rs2);
    const f32x2 n = rbf2(rbf2(n1 - mb2) * rs2);
    constexpr float K1 = -2.f * LOG2E * 0.7978845608028654f, K2 = K1 * 0.044715f;
    float y[2] = {0.f, 0.f};
#pragma unroll
    for (int i = 0; i < 4; ++i)
#pragma unroll
      for (int c = 0; c < 2; ++c) {
        const f32x2 u = rbf2(rbf2(w1f[i] * n[c]) + b1f[i]);
        const f32x2 a = u * (u * u * K2 + K1);
        const f32x2 e = f32x2{__builtin_amdgcn_exp2f(a[0]), __builtin_amdgcn_exp2f(a[1])} + 1.f;
        const f32x2 gl = u * f32x2{__builtin_amdgcn_rcpf(e[0]), __builtin_amdgcn_rcpf(e[1])};
        y[c] = dot2(cvtpk(gl[0], gl[1]), w2v[i], y[c]);
      }
    wave_sum2_l63(y[0], y[1]);
    if (lane == 63) *(f32x2*)(lds + L_RED + 2 * w) = f32x2{y[0], y[1]};
    __syncthreads();
    if (tid == 0) {
      const f32x4 ta = *(const f32x4*)(lds + L_RED), tb = *(const f32x4*)(lds + L_RED + 4);
      const f32x2 ysum = (f32x2{ta[0], ta[1]} + f32x2{ta[2], ta[3]}) + (f32x2{tb[0], tb[1]} + f32x2{tb[2], tb[3]});
      const f32x2 r = hc + rbf2(rbf2(ysum) + bf2f(b216));
      *(unsigned*)(out + c0) = cvtpk(r[0], r[1]);
      if (blockIdx.x == 0) __hip_atomic_store(ws + G_EPOCH, ((u64)cnt[5] << 32) | tag, RLX_AGENT);
    }
  }
  STAMP(10);
}

extern "C" void kernel_launch(void* const* d_in, const int* in_sizes, int n_in, void* d_out, int out_size, void* d_ws, size_t ws_size,
                              hipStream_t stream) {
  k_fused<<<NWG, NTHR, 0, stream>>>((const u16*)d_in[0], (const u16*)d_in[1], (const u16*)d_in[2], (const u16*)d_in[3], (u64*)d_ws, (const u16*)d_in[4],
                                    (const u16*)d_in[6], (const u16*)d_in[7], (const u16*)d_in[8], (const u16*)d_in[9], (u16*)d_out);
}
```

```cpp
#include <hip/hip_runtime.h>
#include <hip/hip_bf16.h>
#include <cstdint>
#include <type_traits>

typedef unsigned v4u __attribute__((ext_vector_type(4)));
typedef unsigned v2u __attribute__((ext_vector_type(2)));
typedef unsigned v16u __attribute__((ext_vector_type(16)));
typedef float f32x4 __attribute__((ext_vector_type(4)));
typedef float f32x2 __attribute__((ext_vector_type(2)));
typedef __bf16 bf16x8 __attribute__((ext_vector_type(8)));
typedef __bf16 bf16x2 __attribute__((ext_vector_type(2)));
typedef unsigned short u16;
typedef unsigned long long u64;
typedef __amdgpu_buffer_rsrc_t rsrc_t;
#define GAS __attribute__((address_space(1)))
#define LAS __attribute__((address_space(3)))
typedef GAS u64 gu64;
#define RLX_AGENT __ATOMIC_RELAXED, __HIP_MEMORY_SCOPE_AGENT

constexpr int D = 512, SK = 65536, DH = 128, FF = 1792;
constexpr int NWAVE = 8, NTHR = 512, NWG = 256;
constexpr int WK = 48;
constexpr int WV = 24;
constexpr int PRE = 4;
constexpr int PW = 7;
constexpr int QPER = 8;
constexpr int TQ = 6;
constexpr int TE = 67, TO = 128 + TQ / 2 - TE;
static_assert(TO <= TE && TQ % 2 == 0, "the 32 workgroups of a kv head cover its 2048 tiles");
constexpr int NTILE = (TE + 7) / 8;
constexpr int NLK = NTILE * 8;
constexpr int NL = 2 * NLK;
constexpr float EPS = 1e-5f;
constexpr float SCALE = 0.08838834764831845f;
constexpr float LOG2E = 1.4426950408889634f;
constexpr int SPIN_CAP = 1 << 19;

constexpr int G_EPOCH = 0;
constexpr int G_Q = 512;
constexpr int G_ML = G_Q + 8 * 256;
constexpr int G_Z = G_ML + 8 * 4 * 32 * 2;
constexpr int G_ACC = G_Z + 16 * 512;
constexpr int G_END = G_ACC + 256 * 512;

__device__ __forceinline__ float bf2f(u16 b) { return __uint_as_float((unsigned)b << 16); }
__device__ __forceinline__ float rbf(float x) { return (float)(__bf16)x; }
__device__ __forceinline__ float asf(unsigned w) { return __uint_as_float(w); }
__device__ __forceinline__ unsigned cvtpk(float lo, float hi) {
  bf16x2 r = {(__bf16)lo, (__bf16)hi};
  return __builtin_bit_cast(unsigned, r);
}
__device__ __forceinline__ f32x2 unpk(unsigned w) { return f32x2{asf(w << 16), asf(w & 0xffff0000u)}; }
__device__ __forceinline__ f32x2 unpk2(unsigned p) {
  return f32x2{asf(__builtin_amdgcn_perm(p, p, 0x01000c0cu)), asf(__builtin_amdgcn_perm(p, p, 0x03020c0cu))};
}
__device__ __forceinline__ f32x2 rbf2(f32x2 v) { return unpk2(cvtpk(v[0], v[1])); }
__device__ __forceinline__ float dot2(unsigned a, unsigned b, float c) {
  return __builtin_amdgcn_fdot2_f32_bf16(__builtin_bit_cast(bf16x2, a), __builtin_bit_cast(bf16x2, b), c, false);
}
__device__ __forceinline__ f32x4 mfma(v4u a, v4u b, f32x4 c) {
  return __builtin_amdgcn_mfma_f32_16x16x32_bf16(__builtin_bit_cast(bf16x8, a), __builtin_bit_cast(bf16x8, b), c, 0, 0, 0);
}
template <int CTRL> __device__ __forceinline__ float dpp(float x) {
  return __builtin_bit_cast(float, __builtin_amdgcn_mov_dpp(__builtin_bit_cast(int, x), CTRL, 0xf, 0xf, true));
}
constexpr int XOR1 = 0xB1, XOR2 = 0x4E, ROR8 = 0x128, ROR4 = 0x124, ROR12 = 0x12C;
__device__ __forceinline__ float swap32_add(float a, float b) {
  auto s = __builtin_amdgcn_permlane32_swap(__float_as_uint(a), __float_as_uint(b), false, false);
  return asf(s[0]) + asf(s[1]);
}
__device__ __forceinline__ float swap16_add(float a, float b) {
  auto s = __builtin_amdgcn_permlane16_swap(__float_as_uint(a), __float_as_uint(b), false, false);
  return asf(s[0]) + asf(s[1]);
}
__device__ __forceinline__ float swap16_max(float x) {
  auto s = __builtin_amdgcn_permlane16_swap(__float_as_uint(x), __float_as_uint(x), false, false);
  return fmaxf(asf(s[0]), asf(s[1]));
}
__device__ __forceinline__ float xrow_sum(float x) { x = swap16_add(x, x); return swap32_add(x, x); }
__device__ __forceinline__ float xrow_max(float x) {
  x = swap16_max(x);
  auto t = __builtin_amdgcn_permlane32_swap(__float_as_uint(x), __float_as_uint(x), false, false);
  return fmaxf(asf(t[0]), asf(t[1]));
}
__device__ __forceinline__ float rdlane(float x, int l) { return __builtin_bit_cast(float, __builtin_amdgcn_readlane(__builtin_bit_cast(int, x), l)); }
#define WS2(C) "v_add_f32_dpp %0, %0, %0 " C "\n\tv_add_f32_dpp %1, %1, %1 " C "\n\ts_nop 0\n\t"
__device__ __forceinline__ void wave_sum2_l63(float& a, float& b) {
  asm volatile("s_nop 2\n\t"
      WS2("quad_perm:[1,0,3,2] row_mask:0xf bank_mask:0xf") WS2("quad_perm:[2,3,0,1] row_mask:0xf bank_mask:0xf")
      WS2("row_ror:4 row_mask:0xf bank_mask:0xf") WS2("row_ror:8 row_mask:0xf bank_mask:0xf")
      WS2("row_bcast:15 row_mask:0xa bank_mask:0xf") WS2("row_bcast:31 row_mask:0xc bank_mask:0xf")
      : "+v"(a), "+v"(b));
}
__device__ __forceinline__ void ln_finish(float s1, float s2, float c, float& mb, float& rs) {
  wave_sum2_l63(s1, s2);
  const float dm = rdlane(s1, 63) * (1.f / D);
  const float var = fmaxf(rdlane(s2, 63) * (1.f / D) - dm * dm, 0.f);
  const f32x2 mv = rbf2(f32x2{c + dm, var});
  mb = mv[0];
  rs = __builtin_amdgcn_rcpf(rbf(__builtin_amdgcn_sqrtf(rbf(mv[1] + EPS))));
}
__device__ __forceinline__ void ln_stats(const f32x2 (&v)[4], float& mb, float& rs) {
  const float c = rdlane(v[0][0], 0);
  const f32x2 d0 = v[0] - c, d1 = v[1] - c, d2 = v[2] - c, d3 = v[3] - c;
  const f32x2 s1 = (d0 + d1) + (d2 + d3), s2 = (d0 * d0 + d1 * d1) + (d2 * d2 + d3 * d3);
  ln_finish(s1[0] + s1[1], s2[0] + s2[1], c, mb, rs);
}
__device__ __forceinline__ unsigned ln_apply_pk(f32x2 v, float mb, float rs) { const f32x2 t = rbf2(v - mb) * rs; return cvtpk(t[0], t[1]); }

constexpr int AUX_SC1 = 16, AUX_NT = 2;
__device__ __forceinline__ v4u get2(rsrc_t rs, int voff, int soff) { return __builtin_amdgcn_raw_buffer_load_b128(rs, voff, soff, AUX_SC1); }
__device__ __forceinline__ void put2(rsrc_t rs, int voff, int soff, float a, float b, unsigned tag) {
  __builtin_amdgcn_raw_buffer_store_b128(v4u{tag, __float_as_uint(a), tag, __float_as_uint(b)}, rs, voff + soff, 0, AUX_SC1);
}
__device__ __forceinline__ unsigned stale(v4u g, unsigned tag) { return (g[0] ^ tag) | (g[2] ^ tag); }
__device__ __forceinline__ bool any(unsigned bad) {
  asm volatile("" : "+v"(bad));
  return __builtin_amdgcn_ballot_w64(bad != 0u) != 0ull;
}
template <int I, int N> struct Loop {
  template <class F> static __device__ __forceinline__ void run(F&& f) {
    if constexpr (I < N) { f(std::integral_constant<int, I>{}); Loop<I + 1, N>::run(f); }
  }
};
struct G16 { v16u a, b, c, d; };
#define G16_LD(S, J) ".set gi_, " #J "\n\t.rept 4\n\tbuffer_load_dwordx4 v[128+4*gi_:131+4*gi_], %[vo], %[rs], " S " offen offset:(gi_&3)*1024 sc1\n\t.set gi_, gi_+1\n\t.endr\n\t"
#define G16_OUT(g) "={v[128:143]}"(g.a), "={v[144:159]}"(g.b), "={v[160:175]}"(g.c), "={v[176:191]}"(g.d)
#define G16_INOUT(g) "+{v[128:143]}"(g.a), "+{v[144:159]}"(g.b), "+{v[160:175]}"(g.c), "+{v[176:191]}"(g.d)
__device__ __forceinline__ void g16_issue(G16& g, rsrc_t rs, int voff, int s0) {
  asm volatile("s_nop 4\n\t" G16_LD("%[s0]", 0) G16_LD("%[s1]", 4) G16_LD("%[s2]", 8) G16_LD("%[s3]", 12)
               : G16_OUT(g)
               : [vo] "{v125}"(voff), [rs] "s"(rs), [s0] "s"(s0), [s1] "s"(s0 + 4096), [s2] "s"(s0 + 8192), [s3] "s"(s0 + 12288)
               : "memory");
}
__device__ __forceinline__ v4u g16_issue1(G16& g, rsrc_t rs, int voff, int s0, int voff2, int soff2) {
  v4u m;
  asm volatile("s_nop 4\n\tbuffer_load_dwordx4 %[m], %[v2], %[rs], %[s4] offen sc1\n\t"
               G16_LD("%[s0]", 0) G16_LD("%[s1]", 4) G16_LD("%[s2]", 8) G16_LD("%[s3]", 12) "s_waitcnt vmcnt(16)"
               : G16_OUT(g), [m] "=&v"(m)
               : [vo] "{v125}"(voff), [v2] "{v124}"(voff2), [rs] "s"(rs), [s0] "s"(s0), [s1] "s"(s0 + 4096), [s2] "s"(s0 + 8192), [s3] "s"(s0 + 12288), [s4] "s"(soff2)
               : "memory");
  return m;
}
#define G16_QUAD(S, Q) \
  "s_bitcmp0_b32 %[pd], " #Q "\n\ts_cbranch_scc1 4f\n\t" \
  ".set gi_, 0\n\t.rept 4\n\tv_bitop3_b32 v[120+gi_], v[128+16*" #Q "+4*gi_], v[130+16*" #Q "+4*gi_], %[tg] bitop3:0x7e\n\t.set gi_, gi_+1\n\t.endr\n\t" \
  "v_or3_b32 v124, v120, v121, v122\n\tv_or_b32_e32 v124, v124, v123\n\tv_cmp_ne_u32_e32 vcc, 0, v124\n\ts_cbranch_vccnz 3f\n\t" \
  "s_bitset0_b32 %[pd], " #Q "\n\ts_branch 4f\n\t" \
  "3:\n\t" \
  ".set gi_, 0\n\t.rept 4\n\tv_cmpx_ne_u32_e32 vcc, 0, v[120+gi_]\n\tbuffer_load_dwordx4 v[128+16*" #Q "+4*gi_:131+16*" #Q "+4*gi_], %[vo], %[rs], " S " offen offset:gi_*1024 sc1\n\ts_mov_b64 exec, -1\n\t.set gi_, gi_+1\n\t.endr\n\t" \
  "4:\n\t"
__device__ __forceinline__ void g16_settle(G16& g, rsrc_t rs, int voff, int s0, unsigned tag) {
  unsigned spin = SPIN_CAP, pend = 15u;
  asm volatile(
      "s_nop 4\n\t"
      "1:\n\t"
      "s_waitcnt vmcnt(0)\n\t"
      G16_QUAD("%[s0]", 0) G16_QUAD("%[s1]", 1) G16_QUAD("%[s2]", 2) G16_QUAD("%[s3]", 3)
      "s_cmp_eq_u32 %[pd], 0\n\t"
      "s_cbranch_scc1 2f\n\t"
      "s_sleep 1\n\t"
      "s_sub_u32 %[sp], %[sp], 1\n\t"
      "s_cbranch_scc0 1b\n\t"
      "s_waitcnt vmcnt(0)\n\t"
      "2:"
      : G16_INOUT(g), [sp] "+s"(spin), [pd] "+s"(pend)
      : [vo] "{v125}"(voff), [rs] "s"(rs), [s0] "s"(s0), [s1] "s"(s0 + 4096), [s2] "s"(s0 + 8192), [s3] "s"(s0 + 12288), [tg] "s"(tag)
      : "v120", "v121", "v122", "v123", "v124", "vcc", "scc", "memory");
}
template <int I, int K> __device__ __forceinline__ float g16_val(const G16& g) {
  const v16u& q = I < 4 ? g.a : I < 8 ? g.b : I < 12 ? g.c : g.d;
  return asf(q[4 * (I & 3) + 1 + 2 * K]);
}
#define GQ_STEP(A, B) \
  "buffer_load_dwordx4 v[" #A ":" #A "+3], %[vo], %[rs], %[s0] offen sc1\n\t" \
  "buffer_load_dwordx4 v[" #A "+4:" #A "+7], %[vo], %[rs], %[s0] offen offset:1024 sc1\n\t" \
  "s_sleep %[sl]\n\t" \
  "s_waitcnt vmcnt(6)\n\t" \
  "v_bitop3_b32 v124, v[" #B "], v[" #B "+2], %[tg] bitop3:0x7e\n\t" \
  "v_bitop3_b32 v123, v[" #B "+4], v[" #B "+6], %[tg] bitop3:0x7e\n\t" \
  "v_mov_b32 v161, v[" #B "+1]\n\tv_mov_b32 v163, v[" #B "+3]\n\tv_mov_b32 v165, v[" #B "+5]\n\tv_mov_b32 v167, v[" #B "+7]\n\t" \
  "v_or_b32_e32 v124, v124, v123\n\t" \
  "v_cmp_ne_u32_e32 vcc, 0, v124\n\t" \
  "s_cbranch_vccz 2f\n\t"
__device__ __forceinline__ void gather_q(v4u (&g)[2], v16u (&fly)[2], rsrc_t rs, int voff, int soff, unsigned tag) {
  unsigned spin = SPIN_CAP;
  const int s0 = __builtin_amdgcn_readfirstlane(soff);
  asm volatile(
      "s_nop 4\n\t"
      "v_not_b32 v136, %[tg]\n\tv_not_b32 v144, %[tg]\n\tv_not_b32 v152, %[tg]\n\t"
      "1:\n\t"
      GQ_STEP(128, 136) GQ_STEP(136, 144) GQ_STEP(144, 152) GQ_STEP(152, 128)
      "s_sub_u32 %[sp], %[sp], 1\n\t"
      "s_cbranch_scc0 1b\n\t"
      "2:"
      : "={v[160:163]}"(g[0]), "={v[164:167]}"(g[1]), "={v[128:143]}"(fly[0]), "={v[144:159]}"(fly[1]), [sp] "+s"(spin)
      : [vo] "{v125}"(voff), [rs] "s"(rs), [s0] "s"(s0), [tg] "s"(tag), [sl] "i"(QPER)
      : "v123", "v124", "vcc", "scc", "memory");
}
__device__ __forceinline__ void gather_q_drain(v16u (&fly)[2]) {
  asm volatile("s_waitcnt vmcnt(0)" :: "{v[128:143]}"(fly[0]), "{v[144:159]}"(fly[1]) : "memory");
}

struct Tiles { int a, b, nt, end; };
template <int I> __device__ __forceinline__ v4u ldkv(rsrc_t rk, rsrc_t rv, int koff, int voff, const Tiles& tl) {
  constexpr int J = I < NLK ? I : I - NLK, k = J >> 3;
  int so = (k >> 1) * 16384 + ((k & 1) ? tl.b : tl.a);
  if (k >= (TO - TQ) / 8 && k >= tl.nt) so = tl.end;
  const v4u r = __builtin_amdgcn_raw_buffer_load_b128(I < NLK ? rk : rv, (I < NLK ? koff : voff) + (J & 7) * 256, so, AUX_NT);
  __builtin_amdgcn_sched_barrier(0x78f);
  return r;
}
template <int BM> __device__ __forceinline__ float bsel(float old, float src) {
  return __builtin_bit_cast(float, __builtin_amdgcn_update_dpp(__builtin_bit_cast(int, old), __builtin_bit_cast(int, src), 0xE4, 0xf, BM, false));
}
#define NTL(p) __builtin_nontemporal_load(p)

constexpr int L_PART = 0;
constexpr int L_RED = L_PART + 8 * 512;
constexpr int L_WT = L_RED + 8 * 64;
constexpr int L_OB = L_WT + 4 * 32;
constexpr int L_MX = L_WT + 8 * 32;
constexpr int L_LS = L_MX + 32;
constexpr int L_SC = L_LS + 32;
constexpr int L_CNT = L_SC + 32;
constexpr int L_Q = L_CNT + 8;
constexpr int L_H = L_Q + 256;
constexpr int L_END = L_H + 512;

__global__ void __launch_bounds__(NTHR, 2) k_fused(const u16* __restrict__ x, const u16* __restrict__ kc, const u16* __restrict__ vc,
                                                   const u16* __restrict__ wq, u64* __restrict__ wsp, const u16* __restrict__ wo,
                                                   const u16* __restrict__ w1, const u16* __restrict__ b1, const u16* __restrict__ w2,
                                                   const u16* __restrict__ b2, u16* __restrict__ out) {
  __shared__ __attribute__((aligned(16))) float lds[L_END];
  gu64* ws = (gu64*)wsp;
  const int tid = threadIdx.x, lane = tid & 63, w = __builtin_amdgcn_readfirstlane(tid >> 6);
  const int par = blockIdx.x & 1, jx = blockIdx.x >> 3;
  const int P = (blockIdx.x >> 1) & 3, i4 = jx & 3;
  const int g = jx >> 2, s = 16 * par + 4 * P + i4;
  __builtin_amdgcn_s_setprio(1);
  volatile LAS unsigned* cnt = (volatile LAS unsigned*)(lds + L_CNT);
  if (tid < 4) cnt[tid] = 0u;
  asm volatile("" :: "s"(x), "s"(kc), "s"(vc), "s"(wq), "s"(wsp));
  unsigned tag;
  const rsrc_t rw = __builtin_amdgcn_make_buffer_rsrc((void*)wsp, 0, G_END * 8, 0x00020000);
  const bool qprod = par && !(jx & 1);
  const int ntw = par ? (qprod ? TO - TQ : TO) : TE, tlo = (ntw * w) >> 3, nt = ((ntw * (w + 1)) >> 3) - tlo;
  const int tile0 = 512 * ((P + g) & 3) + (par ? 4 * TE + (i4 >> 1) * (2 * TO - TQ) + (qprod ? 0 : TO - TQ) : i4 * TE);
  const int qblk = 2 * ((P + (g >> 1)) & 3) + (i4 >> 1);
  const int t0 = tile0 + tlo;
  const Tiles tl{(t0 & 1) * 4096, (t0 & 1) ? 16384 : 4096, nt, (((t0 + nt + 1) >> 1) - (t0 >> 1)) * 16384};
  const size_t key0 = (size_t)g * SK + 64 * (t0 >> 1);
  const int lg = lane >> 4, bank = (lane >> 2) & 3, lh = lane & 3;
  const rsrc_t rk = __builtin_amdgcn_make_buffer_rsrc((void*)(kc + key0 * DH), 0, tl.end, 0x00020000);
  const rsrc_t rv = __builtin_amdgcn_make_buffer_rsrc((void*)(vc + key0 * DH), 0, tl.end, 0x00020000);
  const int koff = (bank & 1) * 8192 + (bank >> 1) * 2048 + 16 * (4 * lg + lh), vvoff = (lg & 1) * 8192 + (lg >> 1) * 2048 + 16 * (lane & 15), voff = 16 * lane;
  v4u buf[NL];
  v4u xv, xa, wv[8];
  v2u tagv, ep;
  if (w == PW) ep = __builtin_amdgcn_raw_buffer_load_b64(rw, 0, G_EPOCH * 8, AUX_SC1);
  if (qprod) {
    const rsrc_t rq = __builtin_amdgcn_make_buffer_rsrc((void*)(wq + (size_t)w * 64 * 4096 + 512 * g + 64 * qblk), 0, 63 * 8192 + 128, 0x00020000);
    xv = NTL((const v4u*)(x + 8 * lane));
    xa = NTL((const v4u*)(x + 64 * w + 8 * (lane >> 3)));
#pragma unroll
    for (int i = 0; i < 8; ++i) wv[i] = __builtin_amdgcn_raw_buffer_load_b128(rq, (lane >> 3) * (8 * 8192) + (lane & 7) * 16, i * 8192, AUX_NT);
    asm volatile("s_barrier" ::: "memory");
  }
  Loop<0, PRE>::run([&](auto I) { buf[I.value] = ldkv<I.value>(rk, rv, koff, vvoff, tl); });
  asm volatile("s_waitcnt lgkmcnt(0)\n\ts_barrier" ::: "memory");
  if (w == PW) {
    __builtin_amdgcn_s_setprio(3);
    const u64 epoch = ((u64)(unsigned)__builtin_amdgcn_readfirstlane(ep[1]) << 32) | (unsigned)__builtin_amdgcn_readfirstlane(ep[0]);
    u64 mixv = epoch + 0x9E3779B97F4A7C15ull;
    mixv = (mixv ^ (mixv >> 30)) * 0xBF58476D1CE4E5B9ull;
    mixv = (mixv ^ (mixv >> 27)) * 0x94D049BB133111EBull;
    mixv ^= mixv >> 31;
    tagv = v2u{(unsigned)mixv == (unsigned)epoch ? (unsigned)mixv ^ 1u : (unsigned)mixv, (unsigned)(mixv >> 32)};
  }
  if (qprod) {
    f32x2 v[4];
#pragma unroll
    for (int i = 0; i < 4; ++i) v[i] = unpk(xv[i]);
    float mb, rs;
    ln_stats(v, mb, rs);
    float a[8] = {0.f, 0.f, 0.f, 0.f, 0.f, 0.f, 0.f, 0.f};
#pragma unroll
    for (int j = 0; j < 4; ++j) {
      const unsigned xn = ln_apply_pk(unpk(xa[j]), mb, rs);
#pragma unroll
      for (int d = 0; d < 4; ++d) {
        a[2 * d] = dot2(__builtin_amdgcn_perm(wv[2 * j + 1][d], wv[2 * j][d], 0x05040100u), xn, a[2 * d]);
        a[2 * d + 1] = dot2(__builtin_amdgcn_perm(wv[2 * j + 1][d], wv[2 * j][d], 0x07060302u), xn, a[2 * d + 1]);
      }
    }
    float c0 = swap16_add(swap32_add(a[0], a[4]), swap32_add(a[2], a[6])), c1 = swap16_add(swap32_add(a[1], a[5]), swap32_add(a[3], a[7]));
    c0 += dpp<ROR8>(c0); c1 += dpp<ROR8>(c1);
    if ((lane & 8) == 0) *(f32x2*)(lds + L_RED + w * 64 + 8 * (lane & 7) + 2 * (lane >> 4)) = f32x2{c0, c1};
    __syncthreads();
    if (w == PW && lane < 16) {
      f32x4 t[8];
#pragma unroll
      for (int i = 0; i < 8; ++i) t[i] = *(const f32x4*)(lds + L_RED + i * 64 + 4 * lane);
      const f32x4 q4 = ((t[0] + t[1]) + (t[2] + t[3])) + ((t[4] + t[5]) + (t[6] + t[7]));
      put2(rw, voff, (G_Q + g * 256 + qblk * 32) * 8, asf(cvtpk(q4[0], q4[1])), asf(cvtpk(q4[2], q4[3])), tagv[0]);
    }
    __builtin_amdgcn_s_barrier();
  }
  if (w == PW) {
    tag = __builtin_amdgcn_readfirstlane(tagv[0]);
    __builtin_amdgcn_s_waitcnt(0x0F70);
    v4u qv[2];
    v16u fly[2];
    gather_q(qv, fly, rw, voff, (G_Q + g * 256) * 8, tag);
#pragma unroll
    for (int i = 0; i < 2; ++i) *(v2u*)(lds + L_Q + 128 * i + 2 * lane) = v2u{qv[i][1], qv[i][3]};
    cnt[4] = tag;
    cnt[5] = tagv[1];
    asm volatile("s_waitcnt lgkmcnt(0)" ::: "memory");
    cnt[1] = 1u;
    __builtin_amdgcn_s_setprio(1);
    gather_q_drain(fly);
  }
  Loop<PRE, WK>::run([&](auto I) {
    buf[I.value] = ldkv<I.value>(rk, rv, koff, vvoff, tl);
  });
#pragma unroll 1
  for (int spin = 0; spin < (SPIN_CAP << 4) && __builtin_amdgcn_readfirstlane(cnt[1]) == 0u; ++spin) __builtin_amdgcn_s_sleep(2);
  asm volatile("" ::: "memory");
  tag = __builtin_amdgcn_readfirstlane(cnt[4]);
  const v4u qb = *(const v4u*)((const u16*)(lds + L_Q) + lh * DH + 8 * (4 * lg + bank));
  __builtin_amdgcn_sched_barrier(0);

  float X[NLK / 4];
  Loop<0, NLK / 4>::run([&](auto T) {
    constexpr int t = T.value;
    f32x4 d[4];
    Loop<0, 4>::run([&](auto M) {
      constexpr int i = 4 * t + M.value;
      d[M.value] = mfma(buf[8 * (t >> 1) + 2 * M.value + (t & 1)], qb, f32x4{0.f, 0.f, 0.f, 0.f});
      if constexpr (i + WK < NLK) buf[i + WK] = ldkv<i + WK>(rk, rv, koff, vvoff, tl);
      if constexpr (i + WK == NLK - 1) {
        if (lane == 0) __hip_atomic_fetch_add((unsigned*)(lds + L_CNT) + 2, 1u, __ATOMIC_RELAXED, __HIP_MEMORY_SCOPE_WORKGROUP);
      }
    });
    float R[4];
#pragma unroll
    for (int k = 0; k < 4; ++k) R[k] = bsel<8>(bsel<4>(bsel<2>(d[k][0], d[(k + 1) & 3][1]), d[(k + 2) & 3][2]), d[(k + 3) & 3][3]);
    X[t] = ((R[0] + dpp<ROR4>(R[1])) + dpp<ROR8>(R[2])) + dpp<ROR12>(R[3]);
    asm volatile("" : "+v"(X[t]));
    __builtin_amdgcn_sched_barrier(0);
  });
  unsigned PK[NLK / 8];
  {
    float m = -INFINITY;
#pragma unroll
    for (int t = 0; t < NLK / 8; ++t) {
      f32x2 sc = rbf2(rbf2(f32x2{X[2 * t], X[2 * t + 1]}) * SCALE);
      if (t >= (TO - TQ) / 8 && t >= nt) sc = f32x2{-INFINITY, -INFINITY};
      X[2 * t] = sc[0]; X[2 * t + 1] = sc[1];
      m = fmaxf(m, fmaxf(sc[0], sc[1]));
    }
    m = fmaxf(m, dpp<ROR4>(m)); m = fmaxf(m, dpp<ROR8>(m)); m = xrow_max(m);
    float l = 0.f;
#pragma unroll
    for (int t = 0; t < NLK / 4; ++t) { X[t] = __builtin_amdgcn_exp2f((X[t] - m) * LOG2E); l += X[t]; }
    l += dpp<ROR4>(l); l += dpp<ROR8>(l); l = xrow_sum(l);
#pragma unroll
    for (int t = 0; t < NLK / 8; ++t) PK[t] = cvtpk(X[2 * t], X[2 * t + 1]);
    unsigned old = 0u;
    if (lane < 4) { lds[L_MX + w * 4 + lane] = m; lds[L_LS + w * 4 + lane] = l; }
    if (lane == 0) old = __hip_atomic_fetch_add((unsigned*)(lds + L_CNT), 1u, __ATOMIC_ACQ_REL, __HIP_MEMORY_SCOPE_WORKGROUP);
    if (__builtin_amdgcn_readfirstlane(old) == NWAVE - 1) {
      const float mw = lds[L_MX + (lane & 31)], lw = lds[L_LS + (lane & 31)];
      float M = fmaxf(mw, dpp<ROR4>(mw)); M = fmaxf(M, dpp<ROR8>(M)); M = swap16_max(M);
      const float sc = __builtin_amdgcn_exp2f((mw - M) * LOG2E);
      float L = lw * sc;
      L += dpp<ROR4>(L); L += dpp<ROR8>(L); L = swap16_add(L, L);
      if (lane < 32) lds[L_SC + lh * 8 + (lane >> 2)] = sc;
      if (lane < 4) put2(rw, lane * 512, (G_ML + g * 256 + s * 2) * 8, M, L, tag);
    }
  }
  const int hp = ((P + g) >> 1) & 1, cbk = 4 * ((P + g) & 1) + i4;
  const rsrc_t ro = __builtin_amdgcn_make_buffer_rsrc((void*)(wo + (size_t)(512 * g + 256 * hp + 64 * (w & 3)) * D + 64 * cbk), 0, par ? 0 : 63 * 1024 + 128, 0x00020000);
  v4u wov[8], w1v, b1v, w2v;
  const unsigned xt = NTL((const unsigned*)(x + 128 * (w & 3) + 2 * lane));
  const u16 b216 = NTL(b2);

#pragma unroll 1
  for (int spin = 0; spin < (SPIN_CAP << 4) && __builtin_amdgcn_readfirstlane(cnt[2]) < NWAVE; ++spin) __builtin_amdgcn_s_sleep(2);
  asm volatile("" ::: "memory");
  __builtin_amdgcn_s_setprio(0);
  Loop<NLK, NLK + WV>::run([&](auto I) { buf[I.value] = ldkv<I.value>(rk, rv, koff, vvoff, tl); });
  unsigned bm[4];
#pragma unroll
  for (int mm = 0; mm < 4; ++mm) bm[mm] = bank == mm ? 0xffffffffu : 0u;
  f32x4 acc[8];
#pragma unroll
  for (int j = 0; j < 8; ++j) acc[j] = f32x4{0.f, 0.f, 0.f, 0.f};
  __builtin_amdgcn_sched_barrier(0);
  Loop<0, NLK / 8>::run([&](auto TT) {
    constexpr int T = TT.value;
    constexpr int base = NLK + 8 * T;
    if (T < (TO - TQ) / 8 || T < nt) {
      v4u pa;
#pragma unroll
      for (int mm = 0; mm < 4; ++mm) pa[mm] = PK[T] & bm[mm];
      if constexpr (T + 1 >= (TO - TQ) / 8 && base + WV >= NL) {
        v4u b[8];
#pragma unroll
        for (int mm = 0; mm < 4; ++mm) {
#pragma unroll
          for (int jj = 0; jj < 4; ++jj) {
            b[2 * jj][mm] = __builtin_amdgcn_perm(buf[base + 2 * mm + 1][jj], buf[base + 2 * mm][jj], 0x05040100u);
            b[2 * jj + 1][mm] = __builtin_amdgcn_perm(buf[base + 2 * mm + 1][jj], buf[base + 2 * mm][jj], 0x07060302u);
          }
          if (mm < 3) __builtin_amdgcn_sched_barrier(0);
        }
#pragma unroll
        for (int j = 0; j < 8; ++j) acc[j] = mfma(pa, b[j], acc[j]);
      } else {
#pragma unroll
        for (int jj = 0; jj < 4; ++jj) {
          v4u b0, b1;
#pragma unroll
          for (int mm = 0; mm < 4; ++mm) {
            b0[mm] = __builtin_amdgcn_perm(buf[base + 2 * mm + 1][jj], buf[base + 2 * mm][jj], 0x05040100u);
            b1[mm] = __builtin_amdgcn_perm(buf[base + 2 * mm + 1][jj], buf[base + 2 * mm][jj], 0x07060302u);
          }
          acc[2 * jj] = mfma(pa, b0, acc[2 * jj]);
          acc[2 * jj + 1] = mfma(pa, b1, acc[2 * jj + 1]);
        }
      }
    }
    __builtin_amdgcn_sched_barrier(0);
    Loop<0, 8>::run([&](auto I) {
      constexpr int idx = base + I.value;
      if constexpr (idx + WV < NL) buf[idx + WV] = ldkv<idx + WV>(rk, rv, koff, vvoff, tl);
    });
    if constexpr (base + WV == NL) {
      if (w < 4) {
#pragma unroll
        for (int i = 0; i < 8; ++i) wov[i] = __builtin_amdgcn_raw_buffer_load_b128(ro, (lane >> 3) * 8192 + (lane & 7) * 16, i * 1024, AUX_NT);
        const int fi = tid < FF / 8 ? tid : 0;
        w1v = NTL((const v4u*)(w1 + 8 * fi)); b1v = NTL((const v4u*)(b1 + 8 * fi)); w2v = NTL((const v4u*)(w2 + 8 * fi));
      }
    }
    __builtin_amdgcn_sched_barrier(0);
  });
  {
    float r2[8];
#pragma unroll
    for (int j = 0; j < 8; ++j) r2[j] = swap16_add(swap32_add(acc[j][0], acc[j][2]), swap32_add(acc[j][1], acc[j][3]));
    *(f32x4*)(lds + L_PART + w * 512 + lane * 8) = f32x4{r2[0], r2[1], r2[2], r2[3]};
    *(f32x4*)(lds + L_PART + w * 512 + lane * 8 + 4) = f32x4{r2[4], r2[5], r2[6], r2[7]};
  }
  if (w >= 4) {
    __syncthreads();
    __builtin_amdgcn_s_barrier();
    if (par == 0) {
      __builtin_amdgcn_s_barrier();
      __builtin_amdgcn_s_barrier();
    }
    G16 zv;
    const int zoff = __builtin_amdgcn_readfirstlane((G_Z + (w & 3) * 2048) * 8);
    g16_issue(zv, rw, voff, zoff);
    LAS f32x2* hw = (LAS f32x2*)(lds + L_H + 128 * (w & 3) + 2 * lane);
    f32x2 xf = unpk(xt);
    asm volatile("" : "+v"(hw), "+v"(xf));
    g16_settle(zv, rw, voff, zoff, tag);
    float ta[8], tb[8];
    Loop<0, 8>::run([&](auto I) {
      constexpr int i = I.value;
      ta[i] = g16_val<2 * i, 0>(zv) + g16_val<2 * i + 1, 0>(zv); tb[i] = g16_val<2 * i, 1>(zv) + g16_val<2 * i + 1, 1>(zv);
    });
    const f32x2 t = {((ta[0] + ta[1]) + (ta[2] + ta[3])) + ((ta[4] + ta[5]) + (ta[6] + ta[7])), ((tb[0] + tb[1]) + (tb[2] + tb[3])) + ((tb[4] + tb[5]) + (tb[6] + tb[7]))};
    *hw = rbf2(xf + rbf2(t));
    __syncthreads();
    return;
  }
  f32x2 w1f[4], b1f[4];
#pragma unroll
  for (int i = 0; i < 4; ++i) { w1f[i] = unpk(w1v[i]); b1f[i] = unpk(b1v[i]); }
  if (tid >= FF / 8) w2v = v4u{0u, 0u, 0u, 0u};
#pragma unroll
  for (int i = 0; i < 4; ++i) asm volatile("" : "+v"(w1f[i]), "+v"(b1f[i]));
  asm volatile("" : "+v"(w2v));
  int poff = (tid >> 5) * 16384 + (tid & 31) * 16 + (G_ACC + g * 32 * 512 + (2 * (s & 15) + par) * 64) * 8;
  asm volatile("" : "+v"(poff));
  auto publish_partial = [&]() {
    __syncthreads();
    const f32x4 sa = *(const f32x4*)(lds + L_SC + w * 8), sb = *(const f32x4*)(lds + L_SC + w * 8 + 4);
    f32x2 o0 = *(const f32x2*)(lds + L_PART + 2 * tid) * sa[0], o1 = *(const f32x2*)(lds + L_PART + 4 * 512 + 2 * tid) * sb[0];
#pragma unroll
    for (int i = 1; i < 4; ++i) {
      o0 += *(const f32x2*)(lds + L_PART + i * 512 + 2 * tid) * sa[i];
      o1 += *(const f32x2*)(lds + L_PART + (4 + i) * 512 + 2 * tid) * sb[i];
    }
    o0 += o1;
    put2(rw, poff, 0, o0[0], o0[1], tag);
    __builtin_amdgcn_s_barrier();
  };
  if (par == 0) {
    v4u wb[8];
#pragma unroll
    for (int jj = 0; jj < 4; ++jj)
#pragma unroll
      for (int mm = 0; mm < 4; ++mm) {
        wb[2 * jj][mm] = __builtin_amdgcn_perm(wov[2 * mm + 1][jj], wov[2 * mm][jj], 0x05040100u);
        wb[2 * jj + 1][mm] = __builtin_amdgcn_perm(wov[2 * mm + 1][jj], wov[2 * mm][jj], 0x07060302u);
      }
#pragma unroll
    for (int i = 0; i < 8; ++i) asm volatile("" : "+v"(wb[i]));
    const int aoff = __builtin_amdgcn_readfirstlane((G_ACC + (g * 8 + hp * 4 + w) * 2048) * 8);
    const int moff = __builtin_amdgcn_readfirstlane((G_ML + (g * 4 + 2 * hp + (w >> 1)) * 64) * 8);
    publish_partial();
    G16 av;
    v4u mv = g16_issue1(av, rw, voff, aoff, (lane & 31) * 16, moff);
#pragma unroll 1
    for (int spin = 0; spin < SPIN_CAP && any(stale(mv, tag)); ++spin) {
      __builtin_amdgcn_s_sleep(8);
      asm volatile("" ::: "memory");
      mv = get2(rw, (lane & 31) * 16, moff);
    }
    f32x4 wt[4];
    {
      const float mp = asf(mv[1]);
      float M = fmaxf(mp, dpp<XOR1>(mp)); M = fmaxf(M, dpp<XOR2>(M)); M = fmaxf(M, dpp<ROR4>(M)); M = fmaxf(M, dpp<ROR8>(M)); M = swap16_max(M);
      const float e = __builtin_amdgcn_exp2f((mp - M) * LOG2E);
      float L = e * asf(mv[3]);
      L += dpp<XOR1>(L); L += dpp<XOR2>(L); L += dpp<ROR4>(L); L += dpp<ROR8>(L); L = swap16_add(L, L);
      lds[L_WT + w * 32 + (lane & 31)] = e * __builtin_amdgcn_rcpf(L);
      asm volatile("" ::: "memory");
#pragma unroll
      for (int i = 0; i < 4; ++i) wt[i] = *(const f32x4*)(lds + L_WT + w * 32 + 16 * (lane >> 5) + 4 * i);
#pragma unroll
      for (int i = 0; i < 4; ++i) asm volatile("" : "+v"(wt[i]));
    }
    LAS u16* obw = (LAS u16*)(lds + L_OB) + 64 * w + 2 * (lane & 31) + (lane >> 5);
    const LAS v4u* obr = (const LAS v4u*)((LAS u16*)(lds + L_OB) + 64 * w + 16 * lg + 8 * (lane & 1));
    LAS f32x4* zw = (LAS f32x4*)(lds + L_RED + w * 128 + (lane & 15) * 8);
    const LAS f32x2* zr = (const LAS f32x2*)(lds + L_RED + 2 * (lane & 31));
    int zpoff = voff + (G_Z + ((cbk >> 1) * 16 + 2 * g + hp) * 128 + (cbk & 1) * 64) * 8;
    asm volatile("" : "+v"(obw), "+v"(obr), "+v"(zw), "+v"(zr), "+v"(zpoff));
    g16_settle(av, rw, voff, aoff, tag);
    float oa[2] = {0.f, 0.f}, ob[2] = {0.f, 0.f};
    Loop<0, 16>::run([&](auto I) {
      constexpr int i = I.value;
      oa[i & 1] = fmaf(g16_val<i, 0>(av), wt[i >> 2][i & 3], oa[i & 1]);
      ob[i & 1] = fmaf(g16_val<i, 1>(av), wt[i >> 2][i & 3], ob[i & 1]);
    });
    const float o = swap32_add(oa[0] + oa[1], ob[0] + ob[1]);
    *obw = (u16)cvtpk(o, o);
    asm volatile("" ::: "memory");
    const v4u af = *obr;
    f32x4 d[8];
#pragma unroll
    for (int j = 0; j < 8; ++j) d[j] = mfma(af, wb[j], f32x4{0.f, 0.f, 0.f, 0.f});
    __builtin_amdgcn_sched_barrier(0);
    if (lane < 16) {
      const bool hi = lane >= 8;
      zw[0] = f32x4{hi ? d[0][1] : d[0][0], hi ? d[1][1] : d[1][0], hi ? d[2][1] : d[2][0], hi ? d[3][1] : d[3][0]};
      zw[1] = f32x4{hi ? d[4][1] : d[4][0], hi ? d[5][1] : d[5][0], hi ? d[6][1] : d[6][0], hi ? d[7][1] : d[7][0]};
    }
    __syncthreads();
    if (tid < 32) {
      f32x2 t[8];
#pragma unroll
      for (int i = 0; i < 8; ++i) t[i] = zr[32 * i];
      const f32x2 zz = ((t[0] + t[1]) + (t[2] + t[3])) + ((t[4] + t[5]) + (t[6] + t[7]));
      put2(rw, zpoff, 0, zz[0], zz[1], tag);
    }
    __builtin_amdgcn_s_barrier();
  } else {
    publish_partial();
  }
  const int c0 = 2 * blockIdx.x;
  const LAS f32x4* hr = (const LAS f32x4*)(lds + L_H + 8 * lane);
  const LAS f32x2* hcr = (const LAS f32x2*)(lds + L_H + c0);
  float b2f = bf2f(b216);
  GAS unsigned* outp = (GAS unsigned*)(out + c0);
  asm volatile("" : "+v"(hr), "+v"(hcr), "+v"(b2f), "+s"(outp));
  {
    u64 pc;
    unsigned clen;
    asm volatile("ktail_begin:\n\ts_getpc_b64 %0\n\ts_mov_b32 %1, ktail_end - ktail_begin" : "=s"(pc), "=s"(clen));
    const unsigned ct = __builtin_amdgcn_raw_buffer_load_b32(__builtin_amdgcn_make_buffer_rsrc((void*)pc, 0, w == 1 && (int)clen > 0 ? clen : 0u, 0x00020000), 128 * lane, 0, 0);
    asm volatile("" :: "v"(ct));
  }
  __syncthreads();
  {
#define STAGE() __builtin_amdgcn_sched_barrier(0)
    const f32x4 ha = hr[0], hb = hr[1];
    const f32x2 hc = *hcr;
    f32x2 v[5] = {f32x2{ha[0], ha[1]}, f32x2{ha[2], ha[3]}, f32x2{hb[0], hb[1]}, f32x2{hb[2], hb[3]}, hc};
    float mb1, rs1, mb2, rs2;
    {
      const f32x2 v4[4] = {v[0], v[1], v[2], v[3]};
      ln_stats(v4, mb1, rs1);
    }
    unsigned y1[5];
#pragma unroll
    for (int i = 0; i < 5; ++i) v[i] -= mb1;
#pragma unroll
    for (int i = 0; i < 5; ++i) y1[i] = cvtpk(v[i][0], v[i][1]);
#pragma unroll
    for (int i = 0; i < 5; ++i) v[i] = unpk2(y1[i]);
    STAGE();
#pragma unroll
    for (int i = 0; i < 5; ++i) v[i] *= rs1;
    STAGE();
#pragma unroll
    for (int i = 0; i < 5; ++i) y1[i] = cvtpk(v[i][0], v[i][1]);
    STAGE();
    float s1 = 0.f, s2 = 0.f;
#pragma unroll
    for (int i = 0; i < 4; ++i) { s1 = dot2(y1[i], 0x3f803f80u, s1); s2 = dot2(y1[i], y1[i], s2); }
    const f32x2 n1 = unpk2(y1[4]);
    ln_finish(s1, s2, 0.f, mb2, rs2);
    const f32x2 n = rbf2(rbf2(n1 - mb2) * rs2);
    constexpr float K1 = -2.f * LOG2E * 0.7978845608028654f, K2 = K1 * 0.044715f;
    f32x2 u[8], a[8];
    unsigned pk[8];
    STAGE();
#pragma unroll
    for (int k = 0; k < 8; ++k) u[k] = w1f[k >> 1] * n[k & 1];
    STAGE();
#pragma unroll
    for (int k = 0; k < 8; ++k) pk[k] = cvtpk(u[k][0], u[k][1]);
    STAGE();
#pragma unroll
    for (int k = 0; k < 8; ++k) u[k] = unpk2(pk[k]);
    STAGE();
#pragma unroll
    for (int k = 0; k < 8; ++k) u[k] += b1f[k >> 1];
    STAGE();
#pragma unroll
    for (int k = 0; k < 8; ++k) pk[k] = cvtpk(u[k][0], u[k][1]);
    STAGE();
#pragma unroll
    for (int k = 0; k < 8; ++k) u[k] = unpk2(pk[k]);
    STAGE();
#pragma unroll
    for (int k = 0; k < 8; ++k) a[k] = u[k] * u[k];
    STAGE();
#pragma unroll
    for (int k = 0; k < 8; ++k) a[k] = a[k] * K2 + K1;
    STAGE();
#pragma unroll
    for (int k = 0; k < 8; ++k) a[k] *= u[k];
    STAGE();
#pragma unroll
    for (int k = 0; k < 8; ++k) a[k] = f32x2{__builtin_amdgcn_exp2f(a[k][0]), __builtin_amdgcn_exp2f(a[k][1])};
    STAGE();
#pragma unroll
    for (int k = 0; k < 8; ++k) a[k] += 1.f;
    STAGE();
#pragma unroll
    for (int k = 0; k < 8; ++k) a[k] = f32x2{__builtin_amdgcn_rcpf(a[k][0]), __builtin_amdgcn_rcpf(a[k][1])};
    STAGE();
#pragma unroll
    for (int k = 0; k < 8; ++k) a[k] *= u[k];
    STAGE();
#pragma unroll
    for (int k = 0; k < 8; ++k) pk[k] = cvtpk(a[k][0], a[k][1]);
    STAGE();
    float y[2] = {0.f, 0.f};
#pragma unroll
    for (int k = 0; k < 8; ++k) y[k & 1] = dot2(pk[k], w2v[k >> 1], y[k & 1]);
    wave_sum2_l63(y[0], y[1]);
    if (lane == 63) *(f32x2*)(lds + L_RED + 2 * w) = f32x2{y[0], y[1]};
    __syncthreads();
    if (tid == 0) {
      const f32x4 ta = *(const f32x4*)(lds + L_RED), tb = *(const f32x4*)(lds + L_RED + 4);
      const f32x2 ysum = (f32x2{ta[0], ta[1]} + f32x2{ta[2], ta[3]}) + (f32x2{tb[0], tb[1]} + f32x2{tb[2], tb[3]});
      const f32x2 r = hc + rbf2(rbf2(ysum) + b2f);
      __hip_atomic_store(outp, cvtpk(r[0], r[1]), RLX_AGENT);
      if (blockIdx.x == 0) __hip_atomic_store(ws + G_EPOCH, ((u64)cnt[5] << 32) | tag, RLX_AGENT);
    }
  }
  asm volatile("ktail_end:");
}

extern "C" void kernel_launch(void* const* d_in, const int* in_sizes, int n_in, void* d_out, int out_size, void* d_ws, size_t ws_size,
                              hipStream_t stream) {
  k_fused<<<NWG, NTHR, 0, stream>>>((const u16*)d_in[0], (const u16*)d_in[1], (const u16*)d_in[2], (const u16*)d_in[3], (u64*)d_ws, (const u16*)d_in[4],
                                    (const u16*)d_in[6], (const u16*)d_in[7], (const u16*)d_in[8], (const u16*)d_in[9], (u16*)d_out);
}
```
